```python
import torch

def _softmax(x, axis=-1):
    return torch.softmax(x, dim=axis)
def _gelu(x):
    return torch.nn.functional.gelu(x, approximate="tanh")
def _silu(x):
    return torch.nn.functional.silu(x)
def _sigmoid(x):
    return torch.sigmoid(x)
def _layernorm(x, axis=-1, eps=1e-5):
    m = x.mean(dim=axis, keepdim=True)
    v = x.var(dim=axis, keepdim=True, unbiased=False)
    return (x - m) / torch.sqrt(v + eps)
def _scatter_add(tgt, idx, src, axis=0):
    return tgt.clone().index_add_(axis, idx, src)
def _segment_sum(src, seg, n):
    out = torch.zeros((n,) + src.shape[1:], dtype=src.dtype)
    return out.index_add_(0, seg, src)
def _pad(x, pw):
    flat = tuple(v for pair in reversed(pw) for v in pair)
    return torch.nn.functional.pad(x, flat)

kernel_name = 'gated_mix__ssm_conv_s4d__dense_activation_squared_relu__prompt'

def setup_inputs():

    _w = (lambda a: (lambda t: t.to(torch.bfloat16) if t.is_floating_point() else t)(torch.as_tensor(a)))
    def _A_setup():
        Bsz, L, D, N = 1, 2048, 512, 128
        return {
            "u": _w((lambda *s: torch.randn(*s, dtype=torch.bfloat16) * 0.05)(Bsz, L, D)),
            "log_A_real": _w((lambda *s: torch.randn(*s, dtype=torch.bfloat16) * 6.4)(D, N) * 0.5),
            "A_imag": _w((lambda *s: torch.randn(*s, dtype=torch.bfloat16) * 0.1)(D, N) * 3.14),
            "C_real": _w((lambda *s: torch.randn(*s, dtype=torch.bfloat16) * 0.4)(D, N) / N ** 0.5),
            "C_imag": _w((lambda *s: torch.randn(*s, dtype=torch.bfloat16) * 0.4)(D, N) / N ** 0.5),
            "log_dt": _w((lambda *s: torch.randn(*s, dtype=torch.bfloat16) * 0.1)(D) * 0.5 - 4.0),
            "D_skip": _w((lambda *s: torch.randn(*s, dtype=torch.bfloat16) * 0.1)(D)),
        }
    _A_in = _A_setup()

    def _B_setup():
        B, S, D, F = 1, 2048, 512, 1792
        return {
            "x": _w((lambda *s: torch.randn(*s, dtype=torch.bfloat16) * 0.05)(B, S, D)),
            "w1": _w((lambda *s: torch.randn(*s, dtype=torch.bfloat16) * 0.05)(D, F)),
            "b1": _w((lambda *s: torch.randn(*s, dtype=torch.bfloat16) * 0.05)(F)),
            "w2": _w((lambda *s: torch.randn(*s, dtype=torch.bfloat16) * 0.05)(F, D)),
            "b2": _w((lambda *s: torch.randn(*s, dtype=torch.bfloat16) * 0.05)(D)),
        }
    _B_in = _B_setup()

    return {
        "A_u": _A_in["u"],
        "A_log_A_real": _A_in["log_A_real"],
        "A_A_imag": _A_in["A_imag"],
        "A_C_real": _A_in["C_real"],
        "A_C_imag": _A_in["C_imag"],
        "A_log_dt": _A_in["log_dt"],
        "A_D_skip": _A_in["D_skip"],
        "B_x": _B_in["x"],
        "B_w1": _B_in["w1"],
        "B_b1": _B_in["b1"],
        "B_w2": _B_in["w2"],
        "B_b2": _B_in["b2"],
        "gate_w": _w((lambda *s: torch.randn(*s, dtype=torch.bfloat16) * 0.05)(512) * 16.0),
    }

def reference(A_u, A_log_A_real, A_A_imag, A_C_real, A_C_imag, A_log_dt, A_D_skip, B_x, B_w1, B_b1, B_w2, B_b2, gate_w):
    def _A_ref(u, log_A_real, A_imag, C_real, C_imag, log_dt, D_skip):
        L = u.shape[1]
        dt = torch.exp(log_dt)
        A = -torch.exp(log_A_real) + 1j * A_imag
        C = (C_real + 1j * C_imag) * dt[:, None]
        pos = (lambda x, dt: x.to(dt))(torch.arange(L), log_dt.dtype)
        P = torch.exp((A * dt[:, None])[:, :, None] * pos[None, None, :])
        K = 2.0 * torch.einsum("dn,dnl->dl", C, P).real
        L2 = 2 * L
        u_t = (lambda x, *d: x.permute(*d))(u, 0, 2, 1)
        Kf = (lambda x, *a, **k: torch.fft.rfft(x.float(), *a, **k))(K, L2)
        Uf = (lambda x, *a, **k: torch.fft.rfft(x.float(), *a, **k))(u_t, L2)
        y = (lambda x, *a, **k: torch.fft.irfft(x, *a, **k).to(torch.bfloat16))(Uf * Kf[None, :, :], L2)[:, :, :L]
        y = (lambda x, *d: x.permute(*d))(y, 0, 2, 1)
        return y + u * D_skip[None, None, :]

    def _B_ref(x, w1, b1, w2, b2):
        h = torch.einsum("bsd,df->bsf", x, w1) + b1
        zero = torch.zeros_like(h)
        r = torch.where(h > zero, h, zero)
        h = r * r
        out = torch.einsum("bsf,fd->bsd", h, w2) + b2
        return out

    x = A_u
    g = _sigmoid((x * gate_w).sum(-1, keepdims=True))
    a = _A_ref(x, A_log_A_real, A_A_imag, A_C_real, A_C_imag, A_log_dt, A_D_skip)
    b = _B_ref(x, B_w1, B_b1, B_w2, B_b2)
    return g * a + (1 - g) * b
```

```cpp
#include <hip/hip_runtime.h>
#include <hip/hip_bf16.h>
typedef unsigned short u16;
__device__ __forceinline__ float bf2f(u16 x){ return __uint_as_float(((unsigned)x)<<16); }
__device__ __forceinline__ u16 f2bf(float f){ unsigned u=__float_as_uint(f); return (u16)((u + 0x7FFF + ((u>>16)&1))>>16); }
__device__ __forceinline__ float rbf(float f){ return bf2f(f2bf(f)); }
constexpr int L=2048, D=512, N=128, F=1792;
#define ROUNDPOS 0
__global__ void kgen(const u16* logA, const u16* Aim, const u16* Cre, const u16* Cim, const u16* logdt, float* Kt){
  int d=blockIdx.x;
  __shared__ float ar[N], ai[N], cr[N], ci[N];
  float dt=rbf(expf(bf2f(logdt[d])));
  if(threadIdx.x<N){ int n=threadIdx.x; ar[n]=-rbf(expf(bf2f(logA[d*N+n])))*dt; ai[n]=bf2f(Aim[d*N+n])*dt; cr[n]=bf2f(Cre[d*N+n])*dt; ci[n]=bf2f(Cim[d*N+n])*dt; }
  __syncthreads();
  for(int l=threadIdx.x;l<L;l+=blockDim.x){
    float p = ROUNDPOS ? rbf((float)l) : (float)l; float s=0;
    for(int n=0;n<N;n++){ float e=expf(ar[n]*p); float sn,cs; sn=__sinf(ai[n]*p); cs=__cosf(ai[n]*p); s+=e*(cr[n]*cs-ci[n]*sn);}
    Kt[l*D+d]=2.f*s;
  }
}
__global__ void conv(const u16* u, const float* Kt, float* y){
  int t=blockIdx.x; int d=threadIdx.x; float s=0;
  for(int l=0;l<=t;l++) s+=Kt[l*D+d]*bf2f(u[(t-l)*D+d]);
  y[t*D+d]=s;
}
__global__ void g1(const u16* x,const u16* w1,const u16* b1,u16* h){
  int t=blockIdx.x; __shared__ float xs[D];
  for(int i=threadIdx.x;i<D;i+=blockDim.x) xs[i]=bf2f(x[t*D+i]);
  __syncthreads();
  for(int f=threadIdx.x;f<F;f+=blockDim.x){ float s=0; for(int k=0;k<D;k++) s+=xs[k]*bf2f(w1[k*F+f]);
    float v=rbf(rbf(s)+bf2f(b1[f])); v=v>0?v:0; h[t*F+f]=f2bf(v*v);}
}
__global__ void g2(const u16* x,const u16* h,const u16* w2,const u16* b2,const u16* gw,const u16* Dsk,const float* y,u16* out){
  int t=blockIdx.x; int d=threadIdx.x; __shared__ float hs[F]; __shared__ float red[D];
  for(int i=threadIdx.x;i<F;i+=blockDim.x) hs[i]=bf2f(h[t*F+i]);
  float xv=bf2f(x[t*D+d]); red[d]=rbf(xv*bf2f(gw[d]));
  __syncthreads();
  float gs=0; for(int i=0;i<D;i++) gs+=red[i];
  gs=rbf(gs); float g=rbf(1.f/(1.f+expf(-gs)));
  float s=0; for(int k=0;k<F;k++) s+=hs[k]*bf2f(w2[k*D+d]);
  float b=rbf(rbf(s)+bf2f(b2[d]));
  float a=rbf(rbf(y[t*D+d])+rbf(xv*bf2f(Dsk[d])));
  out[t*D+d]=f2bf(rbf(g*a)+rbf(rbf(1.f-g)*b));
}
extern "C" void kernel_launch(void* const* d_in, const int* in_sizes, int n_in, void* d_out, int out_size, void* d_ws, size_t ws_size, hipStream_t stream){
  const u16* u=(const u16*)d_in[0];
  float* Kt=(float*)d_ws; float* y=Kt+L*D; u16* h=(u16*)(y+L*D);
  kgen<<<D,256,0,stream>>>((const u16*)d_in[1],(const u16*)d_in[2],(const u16*)d_in[3],(const u16*)d_in[4],(const u16*)d_in[5],Kt);
  conv<<<L,D,0,stream>>>(u,Kt,y);
  g1<<<L,256,0,stream>>>(u,(const u16*)d_in[8],(const u16*)d_in[9],h);
  g2<<<L,D,0,stream>>>(u,h,(const u16*)d_in[10],(const u16*)d_in[11],(const u16*)d_in[12],(const u16*)d_in[6],y,(u16*)d_out);
}
```

```cpp
#include <hip/hip_runtime.h>
#include <hip/hip_bf16.h>
typedef unsigned short u16;
__device__ __forceinline__ float bf2f(u16 x){ return __uint_as_float(((unsigned)x)<<16); }
__device__ __forceinline__ u16 f2bf(float f){ unsigned u=__float_as_uint(f); return (u16)((u + 0x7FFF + ((u>>16)&1))>>16); }
__device__ __forceinline__ float rbf(float f){ return bf2f(f2bf(f)); }
constexpr int L=2048, D=512, N=128, F=1792;
#define ROUNDPOS 1
__global__ void kgen(const u16* logA, const u16* Aim, const u16* Cre, const u16* Cim, const u16* logdt, float* Kt){
  int d=blockIdx.x;
  __shared__ float ar[N], ai[N], cr[N], ci[N];
  float dt=rbf(expf(bf2f(logdt[d])));
  if(threadIdx.x<N){ int n=threadIdx.x; ar[n]=-rbf(expf(bf2f(logA[d*N+n])))*dt; ai[n]=bf2f(Aim[d*N+n])*dt; cr[n]=bf2f(Cre[d*N+n])*dt; ci[n]=bf2f(Cim[d*N+n])*dt; }
  __syncthreads();
  for(int l=threadIdx.x;l<L;l+=blockDim.x){
    float p = ROUNDPOS ? rbf((float)l) : (float)l; float s=0;
    for(int n=0;n<N;n++){ float e=expf(ar[n]*p); float sn,cs; sn=__sinf(ai[n]*p); cs=__cosf(ai[n]*p); s+=e*(cr[n]*cs-ci[n]*sn);}
    Kt[l*D+d]=2.f*s;
  }
}
__global__ void conv(const u16* u, const float* Kt, float* y){
  int t=blockIdx.x; int d=threadIdx.x; float s=0;
  for(int l=0;l<=t;l++) s+=Kt[l*D+d]*bf2f(u[(t-l)*D+d]);
  y[t*D+d]=s;
}
__global__ void g1(const u16* x,const u16* w1,const u16* b1,u16* h){
  int t=blockIdx.x; __shared__ float xs[D];
  for(int i=threadIdx.x;i<D;i+=blockDim.x) xs[i]=bf2f(x[t*D+i]);
  __syncthreads();
  for(int f=threadIdx.x;f<F;f+=blockDim.x){ float s=0; for(int k=0;k<D;k++) s+=xs[k]*bf2f(w1[k*F+f]);
    float v=rbf(rbf(s)+bf2f(b1[f])); v=v>0?v:0; h[t*F+f]=f2bf(v*v);}
}
__global__ void g2(const u16* x,const u16* h,const u16* w2,const u16* b2,const u16* gw,const u16* Dsk,const float* y,u16* out){
  int t=blockIdx.x; int d=threadIdx.x; __shared__ float hs[F]; __shared__ float red[D];
  for(int i=threadIdx.x;i<F;i+=blockDim.x) hs[i]=bf2f(h[t*F+i]);
  float xv=bf2f(x[t*D+d]); red[d]=rbf(xv*bf2f(gw[d]));
  __syncthreads();
  float gs=0; for(int i=0;i<D;i++) gs+=red[i];
  gs=rbf(gs); float g=rbf(1.f/(1.f+expf(-gs)));
  float s=0; for(int k=0;k<F;k++) s+=hs[k]*bf2f(w2[k*D+d]);
  float b=rbf(rbf(s)+bf2f(b2[d]));
  float a=rbf(rbf(y[t*D+d])+rbf(xv*bf2f(Dsk[d])));
  out[t*D+d]=f2bf(rbf(g*a)+rbf(rbf(1.f-g)*b));
}

__global__ void chase(const unsigned* p, unsigned* sink, int n){
  unsigned idx=0, acc=0;
  for(int i=0;i<n;i++){ unsigned v=*(const volatile unsigned*)(p+idx*32); acc+=v; idx=(i+1)*1+(v==0x7fffffffu?1:0); idx&=16383; }
  if(acc==0x12345678) *sink=acc;
}
extern "C" void kernel_launch(void* const* d_in, const int* in_sizes, int n_in, void* d_out, int out_size, void* d_ws, size_t ws_size, hipStream_t stream){
  const u16* u=(const u16*)d_in[0];
  float* Kt=(float*)d_ws; float* y=Kt+L*D; u16* h=(u16*)(y+L*D);
  chase<<<1,1,0,stream>>>((const unsigned*)d_out,(unsigned*)d_ws+(100<<20)/4,4000);
  kgen<<<D,256,0,stream>>>((const u16*)d_in[1],(const u16*)d_in[2],(const u16*)d_in[3],(const u16*)d_in[4],(const u16*)d_in[5],Kt);
  conv<<<L,D,0,stream>>>(u,Kt,y);
  g1<<<L,256,0,stream>>>(u,(const u16*)d_in[8],(const u16*)d_in[9],h);
  g2<<<L,D,0,stream>>>(u,h,(const u16*)d_in[10],(const u16*)d_in[11],(const u16*)d_in[12],(const u16*)d_in[6],y,(u16*)d_out);
}
```

```cpp
#include <hip/hip_runtime.h>

typedef unsigned u32;
typedef unsigned short u16;
typedef unsigned long long u64;
typedef u32 u32x2 __attribute__((ext_vector_type(2)));
typedef u32 u32x4 __attribute__((ext_vector_type(4)));
typedef short s16x4 __attribute__((ext_vector_type(4)));
typedef __bf16 bf16x8 __attribute__((ext_vector_type(8)));
typedef __bf16 bf16x2 __attribute__((ext_vector_type(2)));
typedef float f32x2 __attribute__((ext_vector_type(2)));
typedef float f32x4 __attribute__((ext_vector_type(4)));
typedef float f32x16 __attribute__((ext_vector_type(16)));
#define LAS __attribute__((address_space(3)))
#define DEV __device__ __forceinline__

constexpr int L = 2048, D = 512, NS = 128, F = 1792;
constexpr int SC1 = 16;

constexpr u32 WS_F2 = 0;
constexpr u32 WS_F1 = 8192;
constexpr u32 WS_F3 = 8704;
constexpr u32 WS_FEND = 8704 + 8192;
constexpr u32 WS_G = 32768;
constexpr u32 WS_UT = 65536;
constexpr u32 WS_AT = WS_UT + 2097152;
constexpr u32 WS_H = WS_AT + 2097152;
constexpr u32 WS_END = WS_H + 7340032;
constexpr u32 WS_TS = 16u << 20;

constexpr u32 KST = 4224, UST = 4576, TST = 544;
constexpr u32 LD_K = 0;
constexpr u32 LD_U = 4 * KST;
constexpr u32 LD_TAB = LD_U;
constexpr u32 LD_SYNC = 85504;
constexpr u32 LD_B2 = 65536, LD_B0 = 98304, LD_B1 = 131072;
constexpr u32 LD_R1 = 0, LD_R0 = 57344;
constexpr u32 LD_AST = 114688;
constexpr u32 LD_DUMP = 131072;
constexpr u32 LD_XR = 163840 - 4 * 17024;
constexpr u32 LDS_BYTES = 163840;

typedef __amdgpu_buffer_rsrc_t rsrc_t;
DEV rsrc_t mkrsrc(const void* p, u32 bytes) { return __builtin_amdgcn_make_buffer_rsrc((void*)p, 0, bytes, 0x00020000); }
DEV float bf2f(u32 x) { return __uint_as_float(x << 16); }
DEV float bflo(u32 x) { return __uint_as_float(x << 16); }
DEV float bfhi(u32 x) { return __uint_as_float(x & 0xffff0000u); }
DEV u32 pk(float a, float b) { f32x2 v = {a, b}; return __builtin_bit_cast(u32, __builtin_convertvector(v, bf16x2)); }
DEV float rbf(float a) { return bflo(pk(a, a)); }
DEV bf16x8 asfrag(u32x4 v) { return __builtin_bit_cast(bf16x8, v); }
DEV u32x4 lds128(u32 a) { return *(LAS u32x4*)(size_t)a; }
DEV u32x2 lds64v(u32 a) { return *(volatile LAS u32x2*)(size_t)a; }
DEV u32x2 ldstr(u32 a) { return __builtin_bit_cast(u32x2, __builtin_amdgcn_ds_read_tr16_b64_v4i16((LAS s16x4*)(size_t)a)); }
DEV u32x4 cat(u32x2 a, u32x2 b) { return u32x4{a.x, a.y, b.x, b.y}; }
DEV void sts128(u32 a, u32x4 v) { *(LAS u32x4*)(size_t)a = v; }
DEV void sts64(u32 a, u32x2 v) { *(LAS u32x2*)(size_t)a = v; }
DEV void sts32(u32 a, u32 v) { *(LAS u32*)(size_t)a = v; }
DEV void sts16(u32 a, u32 v) { *(LAS u16*)(size_t)a = (u16)v; }
DEV void glds(rsrc_t r, u32 lds, u32 voff, u32 soff) {
  __builtin_amdgcn_raw_ptr_buffer_load_lds(r, (LAS void*)(size_t)lds, 16, voff, soff, 0, 0);
}
#define BAR() do { asm volatile("" ::: "memory"); __builtin_amdgcn_s_barrier(); asm volatile("" ::: "memory"); } while (0)
#define WAITV(n) asm volatile("s_waitcnt vmcnt(" #n ")" ::: "memory")
#define WAITL(n) asm volatile("s_waitcnt lgkmcnt(" #n ")" ::: "memory")
#define MFMA32(a, b, c) __builtin_amdgcn_mfma_f32_32x32x16_bf16(asfrag(a), asfrag(b), c, 0, 0, 0)
#define MFMA16(a, b, c) __builtin_amdgcn_mfma_f32_16x16x32_bf16(asfrag(a), asfrag(b), c, 0, 0, 0)

#define STAMP(i)

DEV void poll_flags(rsrc_t ws, u32 off) {
  for (int n = 0; n < (1 << 15); ++n) {
    asm volatile("" ::: "memory");
    u32 v = __builtin_amdgcn_raw_buffer_load_b32(ws, off, 0, SC1);
    if (__all(v == 1u)) break;
    __builtin_amdgcn_s_sleep(4);
  }
  asm volatile("" ::: "memory");
}
DEV void set_flag(rsrc_t ws, u32 off, int lane) {
  if (lane == 0) __builtin_amdgcn_raw_buffer_store_b32(1u, ws, off, 0, SC1);
}

struct cplx { float r, i; };
DEV cplx cmul(cplx a, cplx b) { return cplx{a.r * b.r - a.i * b.i, a.r * b.i + a.i * b.r}; }
DEV cplx csq(cplx a) { return cplx{a.r * a.r - a.i * a.i, 2.f * a.r * a.i}; }
DEV cplx cexp2(float e2, float rev) {
  float m = __builtin_amdgcn_exp2f(e2), f = __builtin_amdgcn_fractf(rev);
  return cplx{m * __builtin_amdgcn_cosf(f), m * __builtin_amdgcn_sinf(f)};
}

__global__ __launch_bounds__(512, 2) void fused_kernel(
    const u16* __restrict__ x, const u16* __restrict__ logA, const u16* __restrict__ Aim, const u16* __restrict__ Cre,
    const u16* __restrict__ Cim, const u16* __restrict__ logdt, const u16* __restrict__ Dsk, const u16* __restrict__ w1,
    const u16* __restrict__ b1, const u16* __restrict__ w2, const u16* __restrict__ b2, const u16* __restrict__ gw,
    u16* __restrict__ out, char* __restrict__ wsp) {
  extern __shared__ __attribute__((aligned(16))) char smem_[];
  STAMP(0);
  const int tid = threadIdx.x, l = tid & 63;
  const int w = __builtin_amdgcn_readfirstlane(tid >> 6);
  const int b = blockIdx.x, xcd = b & 7, r = b >> 3;
  const bool spare = r >= 28;
  const int panel = (xcd >> 1) * 4 + r / 7, chunk = (xcd & 1) * 7 + r % 7;
  const int mt = xcd * 4 + (r >> 3), nt = r & 7;
  const int pair = xcd * 32 + r;
  const rsrc_t ws = mkrsrc(wsp, WS_END);
  const int hh = l >> 5, blk = (l >> 4) & 1, q4 = (l & 15) >> 2, p4 = l & 3, l15 = l & 15, qq = l >> 4;

  if (tid < 4) sts32(LD_SYNC + tid * 4, 0);
  WAITL(0);
  BAR();

  if (w < 4) {
    const int dl = w >> 1, d = pair * 2 + dl;
    f32x16 acc[2][2];
    {
      const int n = (w & 1) * 64 + l, o = d * NS + n;
      const float la = bf2f(logA[o]), aim = bf2f(Aim[o]), cre = bf2f(Cre[o]), cim = bf2f(Cim[o]);
      const float dt = rbf(expf(bf2f(logdt[d])));
      if (!spare) {
#pragma unroll
        for (int ft = 0; ft < 2; ++ft)
#pragma unroll
          for (int g = 0; g < 4; ++g) {
            const u32x2 v = *(const u32x2*)(b1 + chunk * 128 + (w & 1) * 64 + ft * 32 + g * 8 + hh * 4);
#pragma unroll
            for (int tt = 0; tt < 2; ++tt) {
              acc[ft][tt][4 * g + 0] = bflo(v.x); acc[ft][tt][4 * g + 1] = bfhi(v.x);
              acc[ft][tt][4 * g + 2] = bflo(v.y); acc[ft][tt][4 * g + 3] = bfhi(v.y);
            }
          }
      }
      if (l < 16) sts32(LD_K + w * KST + 4096 + (w & 1) * 4 + l * 4, 0);
      const float ar = -rbf(expf(la)) * dt * 1.4426950408889634f;
      const float ai = aim * dt * 0.15915494309189535f;
      STAMP(1);
      const cplx z1 = cexp2(ar, ai), z16 = cexp2(16.f * ar, 16.f * ai), z256 = cexp2(256.f * ar, 256.f * ai);
      const cplx z2 = csq(z1), z4 = csq(z2), z8 = csq(z4);
      const u32 tb = LD_TAB + dl * 63 * TST + n * 4;
      cplx lo = cplx{1.f, 0.f}, c = cplx{2.f * cre * dt, 2.f * cim * dt}, v = c;
#pragma unroll
      for (int i = 0; i < 16; ++i) {
        sts32(tb + i * TST, pk(lo.r, -lo.i));
        sts32(tb + (16 + i) * TST, pk(v.r, v.i));
        if (i < 15) { lo = i ? cmul(lo, z16) : z16; v = cmul(v, z1); }
      }
      int row = 32;
#pragma unroll
      for (int tau = 1; tau < 8; ++tau) {
        c = cmul(c, z256);
        v = c;
        const int cnt = tau == 1 ? 9 : (tau < 4 ? 5 : 3);
        const cplx z = tau == 1 ? z2 : (tau < 4 ? z4 : z8);
#pragma unroll
        for (int i = 0; i < cnt; ++i) {
          sts32(tb + row * TST, pk(v.r, v.i));
          ++row;
          if (i + 1 < cnt) v = cmul(v, z);
        }
      }
      WAITL(0);
      if (l == 0) *(volatile LAS u32*)(size_t)(LD_SYNC + w * 4) = 1;
      while (*(volatile LAS u32*)(size_t)(LD_SYNC + (w ^ 1) * 4) == 0) {}
      asm volatile("" ::: "memory");
      STAMP(2);
      const u32 tbase = LD_TAB + dl * 63 * TST + qq * 16;
      u32x4 lo8[8];
#pragma unroll
      for (int ks = 0; ks < 8; ++ks) lo8[ks] = lds128(tbase + l15 * TST + ks * 64);
      const int r2 = (l15 + ((l15 >> 1) & 1)) >> 1, r4 = (l15 + 1 + ((l15 >> 2) & 1)) >> 2, r8 = (l15 + 3 + ((l15 >> 3) & 1)) >> 3;
#pragma unroll
      for (int j = 0; j < 4; ++j) {
        const int rowA = j == 0 ? 16 + l15 : (j == 1 ? 32 + r2 : (j == 2 ? 41 + r4 : 46 + r4));
        const int rowB = 51 + 3 * j + r8;
        const u32 a = tbase + ((w & 1) ? rowB : rowA) * TST;
        f32x4 k4 = {0.f, 0.f, 0.f, 0.f};
#pragma unroll
        for (int ks = 0; ks < 8; ++ks) k4 = MFMA16(lds128(a + ks * 64), lo8[ks], k4);
        const int lb = 256 * (4 * (w & 1) + j) + 16 * l15 + 4 * qq;
        const u32 d0 = pk(k4[3], k4[2]), d1 = pk(k4[1], k4[0]);
        const u32 ka = LD_K + dl * 2 * KST + 2 * (2044 - lb);
        sts64(ka, u32x2{d0, d1});
        sts32(ka + KST + 4, d0);
        sts32(ka + KST + 8, d1);
      }
      WAITL(0);
    }
    STAMP(3);
    if (!spare) {
      const int tm = w >> 1, fh = w & 1;
      const u32 xa0 = (64 * tm + (l & 31)) * 128 + ((hh ^ ((l >> 1) & 7)) << 4);
      const u32 wa0 = 16384 + (8 * hh + q4) * 256 + (((8 * fh + 2 * blk + (p4 >> 1)) ^ (q4 << 2)) << 4) + 8 * (p4 & 1);
      u32x4 fa[2][2], fb[2][2];
      auto rd = [&](int set, u32 buf, int s) {
#pragma unroll
        for (int ft = 0; ft < 2; ++ft) {
          const u32 a = buf + (wa0 ^ (ft << 6)) + s * 4096;
          fa[set][ft] = cat(ldstr(a), ldstr(a + 1024));
        }
#pragma unroll
        for (int tt = 0; tt < 2; ++tt) fb[set][tt] = lds128(buf + (xa0 ^ (s << 5)) + tt * 4096);
      };
      auto mm = [&](int set) {
#pragma unroll
        for (int ft = 0; ft < 2; ++ft)
#pragma unroll
          for (int tt = 0; tt < 2; ++tt) acc[ft][tt] = MFMA32(fa[set][ft], fb[set][tt], acc[ft][tt]);
      };
#pragma unroll
      for (int i = 0; i < 2; ++i) { fa[1][i] = u32x4{0, 0, 0, 0}; fb[1][i] = u32x4{0, 0, 0, 0}; }
      u32 buf = LD_B0;
#pragma unroll 1
      for (int s = 0; s < 8; ++s) {
        BAR();
        rd(0, buf, 0); mm(1);
        rd(1, buf, 1); mm(0);
        rd(0, buf, 2); mm(1);
        rd(1, buf, 3); mm(0);
        WAITL(0);
        buf = buf == LD_B1 ? LD_B2 : buf + 32768;
      }
      mm(1);
      STAMP(4);
      const u32 hb = WS_H + ((panel * 2 + tm) * 2 * 112 + 8 * chunk + 4 * fh) * 1024;
#pragma unroll
      for (int tt = 0; tt < 2; ++tt)
#pragma unroll
        for (int ft = 0; ft < 2; ++ft)
#pragma unroll
          for (int s2 = 0; s2 < 2; ++s2) {
            u32x4 o;
#pragma unroll
            for (int j = 0; j < 4; ++j) {
              const float a0 = acc[ft][tt][8 * s2 + 2 * j], a1 = acc[ft][tt][8 * s2 + 2 * j + 1];
              const float r0 = fmaxf(a0, 0.f), r1 = fmaxf(a1, 0.f);
              o[j] = pk(r0 * r0, r1 * r1);
            }
            __builtin_amdgcn_raw_buffer_store_b128(o, ws, l * 16, hb + (tt * 112 + 2 * ft + s2) * 1024, SC1);
          }
    } else {
      BAR();
    }
    STAMP(5);
    BAR();
    STAMP(6);
    {
      const int ih = w & 1;
      const u32 ka = LD_K + dl * 2 * KST + ((l & 1) ? 2 * (30 - 2 * l15) : KST + 2 * (32 - 2 * l15)) + 16 * qq + 256 * ih;
      const u32 ua = LD_U + dl * 2 * UST + ((l & 1) ? 0 : UST) + 64 * (l15 >> 1) + 16 * qq + 256 * ih;
      f32x4 y[8];
#pragma unroll
      for (int t = 0; t < 8; ++t) y[t] = f32x4{0.f, 0.f, 0.f, 0.f};
#pragma unroll
      for (int i = 0; i < 4; ++i) {
        u32x4 kf[8], uf[8];
#pragma unroll
        for (int j = 0; j < 8; ++j) {
          kf[j] = cat(lds64v(ka + 64 * i + 512 * (7 - j)), lds64v(ka + 64 * i + 512 * (7 - j) + 8));
          uf[j] = lds128(ua + 64 * i + 512 * j);
        }
#pragma unroll
        for (int t = 0; t < 8; ++t)
#pragma unroll
          for (int de = 0; de <= t; ++de) y[t] = MFMA16(kf[de], uf[t - de], y[t]);
      }
      STAMP(7);
      if (!spare) {
        WAITV(0);
        const int fb = 2 * chunk + (w & 1);
        set_flag(ws, WS_F2 + ((panel * 2 + (w >> 1)) * 2 + (fb >= 14)) * 128 + (fb >= 14 ? fb - 14 : fb) * 4, l);
      }
      STAMP(8);
      const int c2 = l15 >> 1;
      const u32 da = LD_DUMP + w * 8192 + ((l & 1) * 32 + ((c2 * 4 + qq) ^ ((l & 1) | (c2 & 2)))) * 16;
#pragma unroll
      for (int t = 0; t < 8; ++t) sts128(da + t * 1024, __builtin_bit_cast(u32x4, y[t]));
      WAITL(0);
    }
    BAR();
    STAMP(9);
    {
      const int kh = w >> 1, th = w & 1;
      poll_flags(ws, WS_F2 + (mt * 2 + kh) * 128 + (l < 14 ? l : 13) * 4);
      STAMP(10);
      const u32 hb = WS_H + ((mt * 2 + th) * 112 + 56 * kh) * 1024;
      const u32 wa = kh * 2048 + (4 * hh + q4) * 128 + (((2 * blk + (p4 >> 1)) ^ ((q4 & 2) << 1)) << 4) + 8 * (p4 & 1);
      f32x16 o[2];
#pragma unroll
      for (int i = 0; i < 16; ++i) { o[0][i] = 0.f; o[1][i] = 0.f; }
      u32x4 hf[14];
#pragma unroll
      for (int i = 0; i < 14; ++i) hf[i] = __builtin_amdgcn_raw_buffer_load_b128(ws, l * 16, hb + i * 1024, SC1);
#pragma unroll 1
      for (int qn = 0; qn < 4; ++qn) {
        const u32 reg = ((qn & 1) ? LD_R1 : LD_R0) + wa;
        const u32 nx = qn < 3 ? hb + (qn + 1) * 14336 : WS_END;
#pragma unroll
        for (int i = 0; i < 14; ++i) {
          const u32x4 a0 = cat(ldstr(reg + i * 4096), ldstr(reg + i * 4096 + 1024));
          const u32x4 a1 = cat(ldstr((reg ^ 64) + i * 4096), ldstr((reg ^ 64) + i * 4096 + 1024));
          o[0] = MFMA32(a0, hf[i], o[0]);
          o[1] = MFMA32(a1, hf[i], o[1]);
          hf[i] = __builtin_amdgcn_raw_buffer_load_b128(ws, l * 16, nx + i * 1024, SC1);
        }
        if (qn < 3) BAR();
      }
      STAMP(11);
      const int t = 32 * th + (l & 31);
      const u32 da = LD_DUMP + kh * 16384 + t * 256;
#pragma unroll
      for (int dt = 0; dt < 2; ++dt)
#pragma unroll
        for (int g = 0; g < 4; ++g)
          sts128(da + (((8 * dt + 2 * g + hh) ^ (t & 7)) << 4),
                 __builtin_bit_cast(u32x4, f32x4{o[dt][4 * g], o[dt][4 * g + 1], o[dt][4 * g + 2], o[dt][4 * g + 3]}));
      WAITL(0);
    }
    BAR();
    STAMP(12);
  } else {
    const int v = w - 4;
    const rsrc_t rx = mkrsrc(x, L * D * 2), rw2 = mkrsrc(w2, F * D * 2);
    const u32 w2v = (l >> 3) * 1024 + (((l & 7) ^ (((l >> 4) & 1) << 2)) << 4);
    const u32 w2s = (896 * (v >> 1) + 8 * (v & 1)) * 1024 + nt * 128;
    auto ldw2 = [&](int step) {
      const int qn = step / 14, i = step % 14;
      glds(rw2, ((qn & 1) ? LD_R1 : LD_R0) + i * 4096 + v * 1024, w2v, w2s + step * 16384);
    };
    if (!spare) {
      const rsrc_t rw1 = mkrsrc(w1, D * F * 2);
      const u32 xv0 = (l >> 3) * 1024 + (((l & 7) ^ (l >> 4)) << 4), xv1 = xv0 ^ 64;
      const u32 w1v = (l >> 4) * (F * 2) + (((l & 15) ^ ((l >> 4) << 2)) << 4);
      const int s0 = (r % 7) + 2 * (r / 7) + (xcd >> 1);
      auto issue = [&](int s, u32 buf) {
        const int ks = (s + s0) & 7;
        const u32 xs = (panel * 128 + 32 * v) * 1024 + ks * 128;
        const u32 wsf = (64 * ks + 16 * v) * (F * 2) + chunk * 256;
#pragma unroll
        for (int i = 0; i < 4; ++i) {
          glds(rx, buf + (32 * v + 8 * i) * 128, (i & 1) ? xv1 : xv0, xs + i * 8192);
          glds(rw1, buf + 16384 + (16 * v + 4 * i) * 256, w1v, wsf + i * 4 * (F * 2));
        }
      };
      issue(0, LD_B0);
      issue(1, LD_B1);
      u32 buf = LD_B2;
#pragma unroll 1
      for (int s = 0; s < 6; ++s) {
        WAITV(8);
        BAR();
        if (s == 0) { STAMP(1); }
        issue(s + 2, buf);
        buf = buf == LD_B1 ? LD_B2 : buf + 32768;
      }
      WAITV(8);
      BAR();
      STAMP(2);
#pragma unroll
      for (int s = 23; s < 28; ++s) ldw2(s);
      WAITV(5);
      BAR();
      STAMP(3);
    } else {
      const int t0 = 64 * (xcd * 4 + r - 28) + 16 * v;
      const u32 xr = LD_XR + v * 17024;
#pragma unroll
      for (int i = 0; i < 16; ++i) glds(rx, xr + i * 1056 + (i >> 3) * 128, l * 16, (t0 + i) * 1024);
      u32x4 gf[16];
#pragma unroll
      for (int ks = 0; ks < 16; ++ks) gf[ks] = *(const u32x4*)(gw + 32 * ks + 8 * qq);
      WAITV(0);
      STAMP(1);
      const int rw = 8 * (p4 & 1) + q4;
      const u32 ta = xr + rw * 1056 + (rw >> 3) * 128 + 8 * (2 * qq + (p4 >> 1));
      const u32 uv = (l >> 3) * 16384 + (l & 7) * 16;
#pragma unroll
      for (int n = 0; n < 16; ++n)
        __builtin_amdgcn_raw_buffer_store_b128(cat(ldstr(ta + n * 64), ldstr(ta + n * 64 + 4 * 1056)), ws, uv,
                                               WS_UT + n * 8 * 16384 + t0 * 8, SC1);
      f32x4 s4 = {0.f, 0.f, 0.f, 0.f};
      const u32 ga = xr + l15 * 1056 + (l15 >> 3) * 128 + 16 * qq;
#pragma unroll
      for (int ks = 0; ks < 16; ++ks) s4 = MFMA16(lds128(ga + ks * 64), gf[ks], s4);
      u32x4 g4;
#pragma unroll
      for (int e = 0; e < 4; ++e) g4[e] = __float_as_uint(rbf(1.f / (1.f + expf(-rbf(s4[e])))));
      if (l15 == 0) __builtin_amdgcn_raw_buffer_store_b128(g4, ws, qq * 16, WS_G + t0 * 4, SC1);
      WAITV(0);
      set_flag(ws, WS_F1 + (t0 >> 4) * 4, l);
      STAMP(2);
      BAR();
#pragma unroll
      for (int s = 23; s < 28; ++s) ldw2(s);
      STAMP(3);
    }
    poll_flags(ws, WS_F1 + v * 128 + (l & 31) * 4);
    STAMP(4);
    {
      u32x4 uu[2];
#pragma unroll
      for (int k = 0; k < 2; ++k)
        uu[k] = __builtin_amdgcn_raw_buffer_load_b128(ws, (l >> 1) * 64 + (l & 1) * 16,
                                                      WS_UT + (pair >> 1) * 16384 + (64 * v + 32 * k) * 64 + (pair & 1) * 32, SC1);
      if (l < 28) sts128(LD_U + v * UST + l * 16, u32x4{0, 0, 0, 0});
      if (l == 28 && (v & 1)) sts16(LD_U + v * UST + 448, 0);
#pragma unroll
      for (int k = 0; k < 2; ++k) {
        const u32 a = LD_U + (l & 1) * 2 * UST + 448 + (64 * v + 32 * k + (l >> 1)) * 16;
        sts128(a, uu[k]);
        sts16(a + UST + 2, uu[k].x);
        sts32(a + UST + 4, __builtin_amdgcn_alignbyte(uu[k].y, uu[k].x, 2));
        sts64(a + UST + 8, u32x2{__builtin_amdgcn_alignbyte(uu[k].z, uu[k].y, 2), __builtin_amdgcn_alignbyte(uu[k].w, uu[k].z, 2)});
        sts16(a + UST + 16, uu[k].w >> 16);
      }
    }
#pragma unroll
    for (int s = 0; s < 14; ++s) ldw2(s);
    WAITL(0);
    STAMP(5);
    BAR();
    const int dl = v >> 1, d = pair * 2 + dl;
    u32x4 uk[2];
#pragma unroll
    for (int k = 0; k < 2; ++k) uk[k] = lds128(LD_U + dl * 2 * UST + 448 + (128 * (v & 1) + 64 * k + l) * 16);
    const float dsk = bf2f(Dsk[d]);
    const int c8 = l & 7, tr = 8 * v + (l >> 3);
    const u32x4 b2v = *(const u32x4*)(b2 + nt * 64 + c8 * 8);
    float g[2];
#pragma unroll
    for (int k = 0; k < 2; ++k) g[k] = __uint_as_float(__builtin_amdgcn_raw_buffer_load_b32(ws, (tr + 32 * k) * 4, WS_G + mt * 256, SC1));
    WAITV(0);
    WAITL(0);
    STAMP(6);
    BAR();
    STAMP(7);
#pragma unroll
    for (int s = 14; s < 23; ++s) ldw2(s);
#pragma unroll
    for (int k = 0; k < 2; ++k) {
      const int a = 128 * (v & 1) + 64 * k + l, a5 = a & 31;
      const u32 da = LD_DUMP + dl * 16384 + (a >> 5) * 1024;
      const u32 pe = (a5 ^ ((a5 >> 2) & 2)) * 16, po = 512 + (a5 ^ (((a5 >> 2) & 2) | 1)) * 16;
      const f32x4 e = __builtin_bit_cast(f32x4, lds128(da + pe)) + __builtin_bit_cast(f32x4, lds128(da + 8192 + pe));
      const f32x4 o = __builtin_bit_cast(f32x4, lds128(da + po)) + __builtin_bit_cast(f32x4, lds128(da + 8192 + po));
      u32x4 res;
#pragma unroll
      for (int j = 0; j < 4; ++j) {
        const u32 yy = pk(e[j], o[j]), ss = pk(bflo(uk[k][j]) * dsk, bfhi(uk[k][j]) * dsk);
        res[j] = pk(bflo(yy) + bflo(ss), bfhi(yy) + bfhi(ss));
      }
      __builtin_amdgcn_raw_buffer_store_b128(res, ws, l * 16, WS_AT + d * 4096 + (128 * (v & 1) + 64 * k) * 16, SC1);
    }
    WAITV(0);
    set_flag(ws, WS_F3 + ((v & 1) * 32 + (d >> 4)) * 128 + (d & 15) * 4, l);
    STAMP(8);
    BAR();
#pragma unroll
    for (int s = 28; s < 42; ++s) ldw2(s);
    WAITV(0);
    BAR();
    STAMP(9);
#pragma unroll
    for (int s = 42; s < 56; ++s) ldw2(s);
    poll_flags(ws, WS_F3 + ((mt >> 4) * 32 + nt * 4 + v) * 128 + l15 * 4);
#pragma unroll
    for (int k = 0; k < 2; ++k) {
      const u32x4 av = __builtin_amdgcn_raw_buffer_load_b128(ws, (l >> 3) * 4096 + c8 * 16,
                                                             WS_AT + (nt * 64 + 16 * v + 8 * k) * 4096 + mt * 128, SC1);
      const u32 a = LD_AST + (16 * v + 8 * k + (l >> 3)) * 132 + c8 * 16;
      sts32(a, av.x); sts32(a + 4, av.y); sts32(a + 8, av.z); sts32(a + 12, av.w);
    }
    WAITV(0);
    WAITL(0);
    STAMP(10);
    BAR();
    float P[2][8], og[2];
#pragma unroll
    for (int k = 0; k < 2; ++k) {
      og[k] = rbf(1.f - g[k]);
#pragma unroll
      for (int j = 0; j < 8; ++j)
        P[k][j] = rbf(g[k] * bf2f(*(LAS u16*)(size_t)(LD_AST + (c8 * 8 + j) * 132 + (tr + 32 * k) * 2)));
    }
    STAMP(11);
    BAR();
    STAMP(12);
#pragma unroll
    for (int k = 0; k < 2; ++k) {
      const int t = tr + 32 * k;
      const u32 da = LD_DUMP + t * 256;
      const u32 s0 = ((2 * c8) ^ (t & 7)) << 4, s1 = s0 ^ 16;
      const f32x4 e = __builtin_bit_cast(f32x4, lds128(da + s0)) + __builtin_bit_cast(f32x4, lds128(da + 16384 + s0));
      const f32x4 o = __builtin_bit_cast(f32x4, lds128(da + s1)) + __builtin_bit_cast(f32x4, lds128(da + 16384 + s1));
      u32x4 res;
#pragma unroll
      for (int j = 0; j < 4; ++j) {
        const float v0 = (j < 2 ? e[2 * j] : o[2 * j - 4]) + bflo(b2v[j]), v1 = (j < 2 ? e[2 * j + 1] : o[2 * j - 3]) + bfhi(b2v[j]);
        res[j] = pk(P[k][2 * j] + og[k] * v0, P[k][2 * j + 1] + og[k] * v1);
      }
      *(u32x4*)(out + (size_t)(mt * 64 + t) * D + nt * 64 + c8 * 8) = res;
    }
    STAMP(13);
  }
}

extern "C" void kernel_launch(void* const* d_in, const int* in_sizes, int n_in, void* d_out, int out_size, void* d_ws,
                              size_t ws_size, hipStream_t stream) {
  (void)hipFuncSetAttribute((const void*)fused_kernel, hipFuncAttributeMaxDynamicSharedMemorySize, LDS_BYTES);
  (void)hipMemsetAsync(d_ws, 0, WS_FEND, stream);
  fused_kernel<<<256, 512, LDS_BYTES, stream>>>(
      (const u16*)d_in[0], (const u16*)d_in[1], (const u16*)d_in[2], (const u16*)d_in[3], (const u16*)d_in[4],
      (const u16*)d_in[5], (const u16*)d_in[6], (const u16*)d_in[8], (const u16*)d_in[9], (const u16*)d_in[10],
      (const u16*)d_in[11], (const u16*)d_in[12], (u16*)d_out, (char*)d_ws);
}
```

```cpp
#include <hip/hip_runtime.h>

typedef unsigned u32;
typedef unsigned short u16;
typedef unsigned long long u64;
typedef u32 u32x2 __attribute__((ext_vector_type(2)));
typedef u32 u32x4 __attribute__((ext_vector_type(4)));
typedef short s16x4 __attribute__((ext_vector_type(4)));
typedef __bf16 bf16x8 __attribute__((ext_vector_type(8)));
typedef __bf16 bf16x2 __attribute__((ext_vector_type(2)));
typedef float f32x2 __attribute__((ext_vector_type(2)));
typedef float f32x4 __attribute__((ext_vector_type(4)));
typedef float f32x16 __attribute__((ext_vector_type(16)));
#define LAS __attribute__((address_space(3)))
#define DEV __device__ __forceinline__

constexpr int L = 2048, D = 512, NS = 128, F = 1792;
constexpr int SC1 = 16;

constexpr u32 WS_F2 = 0;
constexpr u32 WS_F1 = 8192;
constexpr u32 WS_F3 = 8704;
constexpr u32 WS_FEND = 8704 + 8192;
constexpr u32 WS_G = 32768;
constexpr u32 WS_UT = 65536;
constexpr u32 WS_AT = WS_UT + 2097152;
constexpr u32 WS_H = WS_AT + 2097152;
constexpr u32 WS_END = WS_H + 7340032;
constexpr u32 WS_TS = 16u << 20;

constexpr u32 KST = 4224, UST = 4576, TST = 544;
constexpr u32 LD_K = 0;
constexpr u32 LD_U = 4 * KST;
constexpr u32 LD_TAB = LD_U;
constexpr u32 LD_SYNC = 85504;
constexpr u32 LD_B2 = 65536, LD_B0 = 98304, LD_B1 = 131072;
constexpr u32 LD_R1 = 0, LD_R0 = 57344;
constexpr u32 LD_AST = 114688;
constexpr u32 LD_DUMP = 131072;
constexpr u32 LD_XR = 163840 - 4 * 17024;
constexpr u32 LDS_BYTES = 163840;

typedef __amdgpu_buffer_rsrc_t rsrc_t;
DEV rsrc_t mkrsrc(const void* p, u32 bytes) { return __builtin_amdgcn_make_buffer_rsrc((void*)p, 0, bytes, 0x00020000); }
DEV float bf2f(u32 x) { return __uint_as_float(x << 16); }
DEV float bflo(u32 x) { return __uint_as_float(x << 16); }
DEV float bfhi(u32 x) { return __uint_as_float(x & 0xffff0000u); }
DEV u32 pk(float a, float b) { f32x2 v = {a, b}; return __builtin_bit_cast(u32, __builtin_convertvector(v, bf16x2)); }
DEV float rbf(float a) { return bflo(pk(a, a)); }
DEV bf16x8 asfrag(u32x4 v) { return __builtin_bit_cast(bf16x8, v); }
DEV u32x4 lds128(u32 a) { return *(LAS u32x4*)(size_t)a; }
DEV u32x2 lds64v(u32 a) { return *(volatile LAS u32x2*)(size_t)a; }
DEV u32x2 ldstr(u32 a) { return __builtin_bit_cast(u32x2, __builtin_amdgcn_ds_read_tr16_b64_v4i16((LAS s16x4*)(size_t)a)); }
DEV u32x4 cat(u32x2 a, u32x2 b) { return u32x4{a.x, a.y, b.x, b.y}; }
DEV void sts128(u32 a, u32x4 v) { *(LAS u32x4*)(size_t)a = v; }
DEV void sts64(u32 a, u32x2 v) { *(LAS u32x2*)(size_t)a = v; }
DEV void sts32(u32 a, u32 v) { *(LAS u32*)(size_t)a = v; }
DEV void sts16(u32 a, u32 v) { *(LAS u16*)(size_t)a = (u16)v; }
DEV void glds(rsrc_t r, u32 lds, u32 voff, u32 soff) {
  __builtin_amdgcn_raw_ptr_buffer_load_lds(r, (LAS void*)(size_t)lds, 16, voff, soff, 0, 0);
}
#define BAR() do { asm volatile("" ::: "memory"); __builtin_amdgcn_s_barrier(); asm volatile("" ::: "memory"); } while (0)
#define WAITV(n) asm volatile("s_waitcnt vmcnt(" #n ")" ::: "memory")
#define WAITL(n) asm volatile("s_waitcnt lgkmcnt(" #n ")" ::: "memory")
#define MFMA32(a, b, c) __builtin_amdgcn_mfma_f32_32x32x16_bf16(asfrag(a), asfrag(b), c, 0, 0, 0)
#define MFMA16(a, b, c) __builtin_amdgcn_mfma_f32_16x16x32_bf16(asfrag(a), asfrag(b), c, 0, 0, 0)

#define STAMP(i)

DEV void poll_flags(rsrc_t ws, u32 off) {
  for (int n = 0; n < (1 << 15); ++n) {
    asm volatile("" ::: "memory");
    u32 v = __builtin_amdgcn_raw_buffer_load_b32(ws, off, 0, SC1);
    if (__all(v == 1u)) break;
    __builtin_amdgcn_s_sleep(4);
  }
  asm volatile("" ::: "memory");
}
DEV void set_flag(rsrc_t ws, u32 off, int lane) {
  if (lane == 0) __builtin_amdgcn_raw_buffer_store_b32(1u, ws, off, 0, SC1);
}

struct cplx { float r, i; };
DEV cplx cmul(cplx a, cplx b) { return cplx{a.r * b.r - a.i * b.i, a.r * b.i + a.i * b.r}; }
DEV cplx csq(cplx a) { return cplx{a.r * a.r - a.i * a.i, 2.f * a.r * a.i}; }
DEV cplx cexp2(float e2, float rev) {
  float m = __builtin_amdgcn_exp2f(e2), f = __builtin_amdgcn_fractf(rev);
  return cplx{m * __builtin_amdgcn_cosf(f), m * __builtin_amdgcn_sinf(f)};
}

__global__ __launch_bounds__(512, 2) void fused_kernel(
    const u16* __restrict__ x, const u16* __restrict__ logA, const u16* __restrict__ Aim, const u16* __restrict__ Cre,
    const u16* __restrict__ Cim, const u16* __restrict__ logdt, const u16* __restrict__ Dsk, const u16* __restrict__ w1,
    const u16* __restrict__ b1, const u16* __restrict__ w2, const u16* __restrict__ b2, const u16* __restrict__ gw,
    u16* __restrict__ out, char* __restrict__ wsp) {
  extern __shared__ __attribute__((aligned(16))) char smem_[];
  STAMP(0);
  const u64 rtA = __builtin_amdgcn_s_memrealtime();
  const int tid = threadIdx.x, l = tid & 63;
  const int w = __builtin_amdgcn_readfirstlane(tid >> 6);
  const int b = blockIdx.x, xcd = b & 7, r = b >> 3;
  const bool spare = r >= 28;
  const int panel = (xcd >> 1) * 4 + r / 7, chunk = (xcd & 1) * 7 + r % 7;
  const int mt = xcd * 4 + (r >> 3), nt = r & 7;
  const int pair = xcd * 32 + r;
  const rsrc_t ws = mkrsrc(wsp, WS_END);
  const int hh = l >> 5, blk = (l >> 4) & 1, q4 = (l & 15) >> 2, p4 = l & 3, l15 = l & 15, qq = l >> 4;

  if (b == 0) { for (int i = tid; i < (int)WS_FEND / 16; i += 512) __builtin_amdgcn_raw_buffer_store_b128(u32x4{0,0,0,0}, ws, i * 16, 0, SC1); }
  while (__builtin_amdgcn_s_memrealtime() - rtA < 1000) __builtin_amdgcn_s_sleep(1);
  if (tid < 4) sts32(LD_SYNC + tid * 4, 0);
  WAITL(0);
  BAR();

  if (w < 4) {
    const int dl = w >> 1, d = pair * 2 + dl;
    f32x16 acc[2][2];
    {
      const int n = (w & 1) * 64 + l, o = d * NS + n;
      const float la = bf2f(logA[o]), aim = bf2f(Aim[o]), cre = bf2f(Cre[o]), cim = bf2f(Cim[o]);
      const float dt = rbf(expf(bf2f(logdt[d])));
      if (!spare) {
#pragma unroll
        for (int ft = 0; ft < 2; ++ft)
#pragma unroll
          for (int g = 0; g < 4; ++g) {
            const u32x2 v = *(const u32x2*)(b1 + chunk * 128 + (w & 1) * 64 + ft * 32 + g * 8 + hh * 4);
#pragma unroll
            for (int tt = 0; tt < 2; ++tt) {
              acc[ft][tt][4 * g + 0] = bflo(v.x); acc[ft][tt][4 * g + 1] = bfhi(v.x);
              acc[ft][tt][4 * g + 2] = bflo(v.y); acc[ft][tt][4 * g + 3] = bfhi(v.y);
            }
          }
      }
      if (l < 16) sts32(LD_K + w * KST + 4096 + (w & 1) * 4 + l * 4, 0);
      const float ar = -rbf(expf(la)) * dt * 1.4426950408889634f;
      const float ai = aim * dt * 0.15915494309189535f;
      STAMP(1);
      const cplx z1 = cexp2(ar, ai), z16 = cexp2(16.f * ar, 16.f * ai), z256 = cexp2(256.f * ar, 256.f * ai);
      const cplx z2 = csq(z1), z4 = csq(z2), z8 = csq(z4);
      const u32 tb = LD_TAB + dl * 63 * TST + n * 4;
      cplx lo = cplx{1.f, 0.f}, c = cplx{2.f * cre * dt, 2.f * cim * dt}, v = c;
#pragma unroll
      for (int i = 0; i < 16; ++i) {
        sts32(tb + i * TST, pk(lo.r, -lo.i));
        sts32(tb + (16 + i) * TST, pk(v.r, v.i));
        if (i < 15) { lo = i ? cmul(lo, z16) : z16; v = cmul(v, z1); }
      }
      int row = 32;
#pragma unroll
      for (int tau = 1; tau < 8; ++tau) {
        c = cmul(c, z256);
        v = c;
        const int cnt = tau == 1 ? 9 : (tau < 4 ? 5 : 3);
        const cplx z = tau == 1 ? z2 : (tau < 4 ? z4 : z8);
#pragma unroll
        for (int i = 0; i < cnt; ++i) {
          sts32(tb + row * TST, pk(v.r, v.i));
          ++row;
          if (i + 1 < cnt) v = cmul(v, z);
        }
      }
      WAITL(0);
      if (l == 0) *(volatile LAS u32*)(size_t)(LD_SYNC + w * 4) = 1;
      while (*(volatile LAS u32*)(size_t)(LD_SYNC + (w ^ 1) * 4) == 0) {}
      asm volatile("" ::: "memory");
      STAMP(2);
      const u32 tbase = LD_TAB + dl * 63 * TST + qq * 16;
      u32x4 lo8[8];
#pragma unroll
      for (int ks = 0; ks < 8; ++ks) lo8[ks] = lds128(tbase + l15 * TST + ks * 64);
      const int r2 = (l15 + ((l15 >> 1) & 1)) >> 1, r4 = (l15 + 1 + ((l15 >> 2) & 1)) >> 2, r8 = (l15 + 3 + ((l15 >> 3) & 1)) >> 3;
#pragma unroll
      for (int j = 0; j < 4; ++j) {
        const int rowA = j == 0 ? 16 + l15 : (j == 1 ? 32 + r2 : (j == 2 ? 41 + r4 : 46 + r4));
        const int rowB = 51 + 3 * j + r8;
        const u32 a = tbase + ((w & 1) ? rowB : rowA) * TST;
        f32x4 k4 = {0.f, 0.f, 0.f, 0.f};
#pragma unroll
        for (int ks = 0; ks < 8; ++ks) k4 = MFMA16(lds128(a + ks * 64), lo8[ks], k4);
        const int lb = 256 * (4 * (w & 1) + j) + 16 * l15 + 4 * qq;
        const u32 d0 = pk(k4[3], k4[2]), d1 = pk(k4[1], k4[0]);
        const u32 ka = LD_K + dl * 2 * KST + 2 * (2044 - lb);
        sts64(ka, u32x2{d0, d1});
        sts32(ka + KST + 4, d0);
        sts32(ka + KST + 8, d1);
      }
      WAITL(0);
    }
    STAMP(3);
    if (!spare) {
      const int tm = w >> 1, fh = w & 1;
      const u32 xa0 = (64 * tm + (l & 31)) * 128 + ((hh ^ ((l >> 1) & 7)) << 4);
      const u32 wa0 = 16384 + (8 * hh + q4) * 256 + (((8 * fh + 2 * blk + (p4 >> 1)) ^ (q4 << 2)) << 4) + 8 * (p4 & 1);
      u32x4 fa[2][2], fb[2][2];
      auto rd = [&](int set, u32 buf, int s) {
#pragma unroll
        for (int ft = 0; ft < 2; ++ft) {
          const u32 a = buf + (wa0 ^ (ft << 6)) + s * 4096;
          fa[set][ft] = cat(ldstr(a), ldstr(a + 1024));
        }
#pragma unroll
        for (int tt = 0; tt < 2; ++tt) fb[set][tt] = lds128(buf + (xa0 ^ (s << 5)) + tt * 4096);
      };
      auto mm = [&](int set) {
#pragma unroll
        for (int ft = 0; ft < 2; ++ft)
#pragma unroll
          for (int tt = 0; tt < 2; ++tt) acc[ft][tt] = MFMA32(fa[set][ft], fb[set][tt], acc[ft][tt]);
      };
#pragma unroll
      for (int i = 0; i < 2; ++i) { fa[1][i] = u32x4{0, 0, 0, 0}; fb[1][i] = u32x4{0, 0, 0, 0}; }
      u32 buf = LD_B0;
#pragma unroll 1
      for (int s = 0; s < 8; ++s) {
        BAR();
        rd(0, buf, 0); mm(1);
        rd(1, buf, 1); mm(0);
        rd(0, buf, 2); mm(1);
        rd(1, buf, 3); mm(0);
        WAITL(0);
        buf = buf == LD_B1 ? LD_B2 : buf + 32768;
      }
      mm(1);
      STAMP(4);
      const u32 hb = WS_H + ((panel * 2 + tm) * 2 * 112 + 8 * chunk + 4 * fh) * 1024;
#pragma unroll
      for (int tt = 0; tt < 2; ++tt)
#pragma unroll
        for (int ft = 0; ft < 2; ++ft)
#pragma unroll
          for (int s2 = 0; s2 < 2; ++s2) {
            u32x4 o;
#pragma unroll
            for (int j = 0; j < 4; ++j) {
              const float a0 = acc[ft][tt][8 * s2 + 2 * j], a1 = acc[ft][tt][8 * s2 + 2 * j + 1];
              const float r0 = fmaxf(a0, 0.f), r1 = fmaxf(a1, 0.f);
              o[j] = pk(r0 * r0, r1 * r1);
            }
            __builtin_amdgcn_raw_buffer_store_b128(o, ws, l * 16, hb + (tt * 112 + 2 * ft + s2) * 1024, SC1);
          }
    } else {
      BAR();
    }
    STAMP(5);
    BAR();
    STAMP(6);
    {
      const int ih = w & 1;
      const u32 ka = LD_K + dl * 2 * KST + ((l & 1) ? 2 * (30 - 2 * l15) : KST + 2 * (32 - 2 * l15)) + 16 * qq + 256 * ih;
      const u32 ua = LD_U + dl * 2 * UST + ((l & 1) ? 0 : UST) + 64 * (l15 >> 1) + 16 * qq + 256 * ih;
      f32x4 y[8];
#pragma unroll
      for (int t = 0; t < 8; ++t) y[t] = f32x4{0.f, 0.f, 0.f, 0.f};
#pragma unroll
      for (int i = 0; i < 4; ++i) {
        u32x4 kf[8], uf[8];
#pragma unroll
        for (int j = 0; j < 8; ++j) {
          kf[j] = cat(lds64v(ka + 64 * i + 512 * (7 - j)), lds64v(ka + 64 * i + 512 * (7 - j) + 8));
          uf[j] = lds128(ua + 64 * i + 512 * j);
        }
#pragma unroll
        for (int t = 0; t < 8; ++t)
#pragma unroll
          for (int de = 0; de <= t; ++de) y[t] = MFMA16(kf[de], uf[t - de], y[t]);
      }
      STAMP(7);
      if (!spare) {
        WAITV(0);
        const int fb = 2 * chunk + (w & 1);
        set_flag(ws, WS_F2 + ((panel * 2 + (w >> 1)) * 2 + (fb >= 14)) * 128 + (fb >= 14 ? fb - 14 : fb) * 4, l);
      }
      STAMP(8);
      const int c2 = l15 >> 1;
      const u32 da = LD_DUMP + w * 8192 + ((l & 1) * 32 + ((c2 * 4 + qq) ^ ((l & 1) | (c2 & 2)))) * 16;
#pragma unroll
      for (int t = 0; t < 8; ++t) sts128(da + t * 1024, __builtin_bit_cast(u32x4, y[t]));
      WAITL(0);
    }
    BAR();
    STAMP(9);
    {
      const int kh = w >> 1, th = w & 1;
      poll_flags(ws, WS_F2 + (mt * 2 + kh) * 128 + (l < 14 ? l : 13) * 4);
      STAMP(10);
      const u32 hb = WS_H + ((mt * 2 + th) * 112 + 56 * kh) * 1024;
      const u32 wa = kh * 2048 + (4 * hh + q4) * 128 + (((2 * blk + (p4 >> 1)) ^ ((q4 & 2) << 1)) << 4) + 8 * (p4 & 1);
      f32x16 o[2];
#pragma unroll
      for (int i = 0; i < 16; ++i) { o[0][i] = 0.f; o[1][i] = 0.f; }
      u32x4 hf[14];
#pragma unroll
      for (int i = 0; i < 14; ++i) hf[i] = __builtin_amdgcn_raw_buffer_load_b128(ws, l * 16, hb + i * 1024, SC1);
#pragma unroll 1
      for (int qn = 0; qn < 4; ++qn) {
        const u32 reg = ((qn & 1) ? LD_R1 : LD_R0) + wa;
        const u32 nx = qn < 3 ? hb + (qn + 1) * 14336 : WS_END;
#pragma unroll
        for (int i = 0; i < 14; ++i) {
          const u32x4 a0 = cat(ldstr(reg + i * 4096), ldstr(reg + i * 4096 + 1024));
          const u32x4 a1 = cat(ldstr((reg ^ 64) + i * 4096), ldstr((reg ^ 64) + i * 4096 + 1024));
          o[0] = MFMA32(a0, hf[i], o[0]);
          o[1] = MFMA32(a1, hf[i], o[1]);
          hf[i] = __builtin_amdgcn_raw_buffer_load_b128(ws, l * 16, nx + i * 1024, SC1);
        }
        if (qn < 3) BAR();
      }
      STAMP(11);
      const int t = 32 * th + (l & 31);
      const u32 da = LD_DUMP + kh * 16384 + t * 256;
#pragma unroll
      for (int dt = 0; dt < 2; ++dt)
#pragma unroll
        for (int g = 0; g < 4; ++g)
          sts128(da + (((8 * dt + 2 * g + hh) ^ (t & 7)) << 4),
                 __builtin_bit_cast(u32x4, f32x4{o[dt][4 * g], o[dt][4 * g + 1], o[dt][4 * g + 2], o[dt][4 * g + 3]}));
      WAITL(0);
    }
    BAR();
    STAMP(12);
  } else {
    const int v = w - 4;
    const rsrc_t rx = mkrsrc(x, L * D * 2), rw2 = mkrsrc(w2, F * D * 2);
    const u32 w2v = (l >> 3) * 1024 + (((l & 7) ^ (((l >> 4) & 1) << 2)) << 4);
    const u32 w2s = (896 * (v >> 1) + 8 * (v & 1)) * 1024 + nt * 128;
    auto ldw2 = [&](int step) {
      const int qn = step / 14, i = step % 14;
      glds(rw2, ((qn & 1) ? LD_R1 : LD_R0) + i * 4096 + v * 1024, w2v, w2s + step * 16384);
    };
    if (!spare) {
      const rsrc_t rw1 = mkrsrc(w1, D * F * 2);
      const u32 xv0 = (l >> 3) * 1024 + (((l & 7) ^ (l >> 4)) << 4), xv1 = xv0 ^ 64;
      const u32 w1v = (l >> 4) * (F * 2) + (((l & 15) ^ ((l >> 4) << 2)) << 4);
      const int s0 = (r % 7) + 2 * (r / 7) + (xcd >> 1);
      auto issue = [&](int s, u32 buf) {
        const int ks = (s + s0) & 7;
        const u32 xs = (panel * 128 + 32 * v) * 1024 + ks * 128;
        const u32 wsf = (64 * ks + 16 * v) * (F * 2) + chunk * 256;
#pragma unroll
        for (int i = 0; i < 4; ++i) {
          glds(rx, buf + (32 * v + 8 * i) * 128, (i & 1) ? xv1 : xv0, xs + i * 8192);
          glds(rw1, buf + 16384 + (16 * v + 4 * i) * 256, w1v, wsf + i * 4 * (F * 2));
        }
      };
      issue(0, LD_B0);
      issue(1, LD_B1);
      u32 buf = LD_B2;
#pragma unroll 1
      for (int s = 0; s < 6; ++s) {
        WAITV(8);
        BAR();
        if (s == 0) { STAMP(1); }
        issue(s + 2, buf);
        buf = buf == LD_B1 ? LD_B2 : buf + 32768;
      }
      WAITV(8);
      BAR();
      STAMP(2);
#pragma unroll
      for (int s = 23; s < 28; ++s) ldw2(s);
      WAITV(5);
      BAR();
      STAMP(3);
    } else {
      const int t0 = 64 * (xcd * 4 + r - 28) + 16 * v;
      const u32 xr = LD_XR + v * 17024;
#pragma unroll
      for (int i = 0; i < 16; ++i) glds(rx, xr + i * 1056 + (i >> 3) * 128, l * 16, (t0 + i) * 1024);
      u32x4 gf[16];
#pragma unroll
      for (int ks = 0; ks < 16; ++ks) gf[ks] = *(const u32x4*)(gw + 32 * ks + 8 * qq);
      WAITV(0);
      STAMP(1);
      const int rw = 8 * (p4 & 1) + q4;
      const u32 ta = xr + rw * 1056 + (rw >> 3) * 128 + 8 * (2 * qq + (p4 >> 1));
      const u32 uv = (l >> 3) * 16384 + (l & 7) * 16;
#pragma unroll
      for (int n = 0; n < 16; ++n)
        __builtin_amdgcn_raw_buffer_store_b128(cat(ldstr(ta + n * 64), ldstr(ta + n * 64 + 4 * 1056)), ws, uv,
                                               WS_UT + n * 8 * 16384 + t0 * 8, SC1);
      f32x4 s4 = {0.f, 0.f, 0.f, 0.f};
      const u32 ga = xr + l15 * 1056 + (l15 >> 3) * 128 + 16 * qq;
#pragma unroll
      for (int ks = 0; ks < 16; ++ks) s4 = MFMA16(lds128(ga + ks * 64), gf[ks], s4);
      u32x4 g4;
#pragma unroll
      for (int e = 0; e < 4; ++e) g4[e] = __float_as_uint(rbf(1.f / (1.f + expf(-rbf(s4[e])))));
      if (l15 == 0) __builtin_amdgcn_raw_buffer_store_b128(g4, ws, qq * 16, WS_G + t0 * 4, SC1);
      WAITV(0);
      set_flag(ws, WS_F1 + (t0 >> 4) * 4, l);
      STAMP(2);
      BAR();
#pragma unroll
      for (int s = 23; s < 28; ++s) ldw2(s);
      STAMP(3);
    }
    poll_flags(ws, WS_F1 + v * 128 + (l & 31) * 4);
    STAMP(4);
    {
      u32x4 uu[2];
#pragma unroll
      for (int k = 0; k < 2; ++k)
        uu[k] = __builtin_amdgcn_raw_buffer_load_b128(ws, (l >> 1) * 64 + (l & 1) * 16,
                                                      WS_UT + (pair >> 1) * 16384 + (64 * v + 32 * k) * 64 + (pair & 1) * 32, SC1);
      if (l < 28) sts128(LD_U + v * UST + l * 16, u32x4{0, 0, 0, 0});
      if (l == 28 && (v & 1)) sts16(LD_U + v * UST + 448, 0);
#pragma unroll
      for (int k = 0; k < 2; ++k) {
        const u32 a = LD_U + (l & 1) * 2 * UST + 448 + (64 * v + 32 * k + (l >> 1)) * 16;
        sts128(a, uu[k]);
        sts16(a + UST + 2, uu[k].x);
        sts32(a + UST + 4, __builtin_amdgcn_alignbyte(uu[k].y, uu[k].x, 2));
        sts64(a + UST + 8, u32x2{__builtin_amdgcn_alignbyte(uu[k].z, uu[k].y, 2), __builtin_amdgcn_alignbyte(uu[k].w, uu[k].z, 2)});
        sts16(a + UST + 16, uu[k].w >> 16);
      }
    }
#pragma unroll
    for (int s = 0; s < 14; ++s) ldw2(s);
    WAITL(0);
    STAMP(5);
    BAR();
    const int dl = v >> 1, d = pair * 2 + dl;
    u32x4 uk[2];
#pragma unroll
    for (int k = 0; k < 2; ++k) uk[k] = lds128(LD_U + dl * 2 * UST + 448 + (128 * (v & 1) + 64 * k + l) * 16);
    const float dsk = bf2f(Dsk[d]);
    const int c8 = l & 7, tr = 8 * v + (l >> 3);
    const u32x4 b2v = *(const u32x4*)(b2 + nt * 64 + c8 * 8);
    float g[2];
#pragma unroll
    for (int k = 0; k < 2; ++k) g[k] = __uint_as_float(__builtin_amdgcn_raw_buffer_load_b32(ws, (tr + 32 * k) * 4, WS_G + mt * 256, SC1));
    WAITV(0);
    WAITL(0);
    STAMP(6);
    BAR();
    STAMP(7);
#pragma unroll
    for (int s = 14; s < 23; ++s) ldw2(s);
#pragma unroll
    for (int k = 0; k < 2; ++k) {
      const int a = 128 * (v & 1) + 64 * k + l, a5 = a & 31;
      const u32 da = LD_DUMP + dl * 16384 + (a >> 5) * 1024;
      const u32 pe = (a5 ^ ((a5 >> 2) & 2)) * 16, po = 512 + (a5 ^ (((a5 >> 2) & 2) | 1)) * 16;
      const f32x4 e = __builtin_bit_cast(f32x4, lds128(da + pe)) + __builtin_bit_cast(f32x4, lds128(da + 8192 + pe));
      const f32x4 o = __builtin_bit_cast(f32x4, lds128(da + po)) + __builtin_bit_cast(f32x4, lds128(da + 8192 + po));
      u32x4 res;
#pragma unroll
      for (int j = 0; j < 4; ++j) {
        const u32 yy = pk(e[j], o[j]), ss = pk(bflo(uk[k][j]) * dsk, bfhi(uk[k][j]) * dsk);
        res[j] = pk(bflo(yy) + bflo(ss), bfhi(yy) + bfhi(ss));
      }
      __builtin_amdgcn_raw_buffer_store_b128(res, ws, l * 16, WS_AT + d * 4096 + (128 * (v & 1) + 64 * k) * 16, SC1);
    }
    WAITV(0);
    set_flag(ws, WS_F3 + ((v & 1) * 32 + (d >> 4)) * 128 + (d & 15) * 4, l);
    STAMP(8);
    BAR();
#pragma unroll
    for (int s = 28; s < 42; ++s) ldw2(s);
    WAITV(0);
    BAR();
    STAMP(9);
#pragma unroll
    for (int s = 42; s < 56; ++s) ldw2(s);
    poll_flags(ws, WS_F3 + ((mt >> 4) * 32 + nt * 4 + v) * 128 + l15 * 4);
#pragma unroll
    for (int k = 0; k < 2; ++k) {
      const u32x4 av = __builtin_amdgcn_raw_buffer_load_b128(ws, (l >> 3) * 4096 + c8 * 16,
                                                             WS_AT + (nt * 64 + 16 * v + 8 * k) * 4096 + mt * 128, SC1);
      const u32 a = LD_AST + (16 * v + 8 * k + (l >> 3)) * 132 + c8 * 16;
      sts32(a, av.x); sts32(a + 4, av.y); sts32(a + 8, av.z); sts32(a + 12, av.w);
    }
    WAITV(0);
    WAITL(0);
    STAMP(10);
    BAR();
    float P[2][8], og[2];
#pragma unroll
    for (int k = 0; k < 2; ++k) {
      og[k] = rbf(1.f - g[k]);
#pragma unroll
      for (int j = 0; j < 8; ++j)
        P[k][j] = rbf(g[k] * bf2f(*(LAS u16*)(size_t)(LD_AST + (c8 * 8 + j) * 132 + (tr + 32 * k) * 2)));
    }
    STAMP(11);
    BAR();
    STAMP(12);
#pragma unroll
    for (int k = 0; k < 2; ++k) {
      const int t = tr + 32 * k;
      const u32 da = LD_DUMP + t * 256;
      const u32 s0 = ((2 * c8) ^ (t & 7)) << 4, s1 = s0 ^ 16;
      const f32x4 e = __builtin_bit_cast(f32x4, lds128(da + s0)) + __builtin_bit_cast(f32x4, lds128(da + 16384 + s0));
      const f32x4 o = __builtin_bit_cast(f32x4, lds128(da + s1)) + __builtin_bit_cast(f32x4, lds128(da + 16384 + s1));
      u32x4 res;
#pragma unroll
      for (int j = 0; j < 4; ++j) {
        const float v0 = (j < 2 ? e[2 * j] : o[2 * j - 4]) + bflo(b2v[j]), v1 = (j < 2 ? e[2 * j + 1] : o[2 * j - 3]) + bfhi(b2v[j]);
        res[j] = pk(P[k][2 * j] + og[k] * v0, P[k][2 * j + 1] + og[k] * v1);
      }
      *(u32x4*)(out + (size_t)(mt * 64 + t) * D + nt * 64 + c8 * 8) = res;
    }
    STAMP(13);
  }
  while (__builtin_amdgcn_s_memrealtime() - rtA < 5000) __builtin_amdgcn_s_sleep(1);
}

extern "C" void kernel_launch(void* const* d_in, const int* in_sizes, int n_in, void* d_out, int out_size, void* d_ws,
                              size_t ws_size, hipStream_t stream) {
  (void)hipFuncSetAttribute((const void*)fused_kernel, hipFuncAttributeMaxDynamicSharedMemorySize, LDS_BYTES);

  fused_kernel<<<256, 512, LDS_BYTES, stream>>>(
      (const u16*)d_in[0], (const u16*)d_in[1], (const u16*)d_in[2], (const u16*)d_in[3], (const u16*)d_in[4],
      (const u16*)d_in[5], (const u16*)d_in[6], (const u16*)d_in[8], (const u16*)d_in[9], (const u16*)d_in[10],
      (const u16*)d_in[11], (const u16*)d_in[12], (u16*)d_out, (char*)d_ws);
}
```

```cpp
#include <hip/hip_runtime.h>

typedef unsigned u32;
typedef unsigned short u16;
typedef unsigned long long u64;
typedef u32 u32x2 __attribute__((ext_vector_type(2)));
typedef u32 u32x4 __attribute__((ext_vector_type(4)));
typedef short s16x4 __attribute__((ext_vector_type(4)));
typedef __bf16 bf16x8 __attribute__((ext_vector_type(8)));
typedef __bf16 bf16x2 __attribute__((ext_vector_type(2)));
typedef float f32x2 __attribute__((ext_vector_type(2)));
typedef float f32x4 __attribute__((ext_vector_type(4)));
typedef float f32x16 __attribute__((ext_vector_type(16)));
typedef u64 u64x2 __attribute__((ext_vector_type(2)));
typedef u64 u64x4 __attribute__((ext_vector_type(4)));
typedef u64 u64x8 __attribute__((ext_vector_type(8)));
#define GAS __attribute__((address_space(1)))
typedef const GAS u16* gp16;
#define LAS __attribute__((address_space(3)))
#define DEV __device__ __forceinline__

constexpr int L = 2048, D = 512, NS = 128, F = 1792;
constexpr int SC1 = 16;
#define CODE_LINES 188

#define W2PF 2
#define MAP2 0
#define ROTC 0
#define SLPF 2
#define WPRIO 0
#define KWAT 8
#define NPA 12
#define NH0 6
#define NP7 4
#define NW0 14
#define X2V 46
#define EARLYW 38
#define F3AT 22
#define ROTA 0
#define ROTB 0
#define NPRE 4
#define POLLC 7
#define FLAGC 104
#define NLATE 0
#define HRING 10

constexpr u32 WS_F2 = 0;
constexpr u32 WS_F3 = 8192;
constexpr u32 WS_F1 = 16384;
constexpr u32 WS_F4 = 24576;
constexpr u32 WS_HB = 32768;
constexpr u32 WS_G = 36864;
constexpr u32 WS_UT = 65536;
constexpr u32 WS_AT = WS_UT + 2097152;
constexpr u32 WS_H = WS_AT + 2097152;
constexpr u32 WS_END = WS_H + 7340032;
constexpr u32 WS_TS = 16u << 20;
constexpr int LEADER = 56;
constexpr int SPIN_MAX = 1 << 16;

constexpr u32 KST = 4224, UST = 4576, TST = 544;
constexpr u32 LD_TAB = 0;
constexpr u32 LD_B0 = 98304;
constexpr u32 LD_K = 98304 - 4 * KST;
constexpr u32 LD_U = 98304;
constexpr u32 LD_DUMP = 131072;
constexpr u32 LD_AST = 65536;
constexpr u32 LDS_BYTES = 163840;
constexpr int NDMA = 22;
constexpr int NPR = 14;
constexpr int X1 = NDMA, X2 = X2V;
constexpr u32 g1buf(int s) { return (98304u + 32768u * (s & 3) + ((s & 2) ? 0u : 0u)) % 163840u; }
constexpr u32 slot(int s) { return 4096u * (s < 19 ? s : s < 22 ? s + 10 : s < 32 ? s - 3 : s < 40 ? s : s - 40); }

typedef __amdgpu_buffer_rsrc_t rsrc_t;
DEV rsrc_t mkrsrc(u64 p, u32 bytes) { return __builtin_amdgcn_make_buffer_rsrc((void*)p, 0, bytes, 0x00020000); }
DEV u32x4 mksrd(u64 a, u32 bytes) { return u32x4{(u32)a, (u32)(a >> 32) & 0xffffu, bytes, 0x00020000u}; }
DEV float bf2f(u32 x) { return __uint_as_float(x << 16); }
DEV float bflo(u32 x) { return __uint_as_float(x << 16); }
DEV float bfhi(u32 x) { return __uint_as_float(x & 0xffff0000u); }
DEV u32 pk(float a, float b) { f32x2 v = {a, b}; return __builtin_bit_cast(u32, __builtin_convertvector(v, bf16x2)); }
DEV float rbf(float a) { return bflo(pk(a, a)); }
DEV bf16x8 asfrag(u32x4 v) { return __builtin_bit_cast(bf16x8, v); }
DEV u32 opq(u32 a) { asm volatile("" : "+v"(a)); return a; }
DEV u32 opqs(u32 a) { asm volatile("" : "+s"(a)); return a; }
DEV u32x4 lds128(u32 a) { return *(LAS u32x4*)(size_t)a; }
DEV u32x2 lds64v(u32 a) { return *(volatile LAS u32x2*)(size_t)a; }
DEV u32x2 ldstr(u32 a) { return __builtin_bit_cast(u32x2, __builtin_amdgcn_ds_read_tr16_b64_v4i16((LAS s16x4*)(size_t)a)); }
DEV u32x4 cat(u32x2 a, u32x2 b) { return u32x4{a.x, a.y, b.x, b.y}; }
DEV void sts128(u32 a, u32x4 v) { *(LAS u32x4*)(size_t)a = v; }
DEV void sts64(u32 a, u32x2 v) { *(LAS u32x2*)(size_t)a = v; }
DEV void sts32(u32 a, u32 v) { *(LAS u32*)(size_t)a = v; }
DEV void sts16(u32 a, u32 v) { *(LAS u16*)(size_t)a = (u16)v; }
DEV void glds(u32x4 srd, u32 lds, u32 voff, u32 soff) {
  asm volatile("s_mov_b32 m0, %0\n\ts_nop 0\n\tbuffer_load_dwordx4 %1, %2, %3 offen lds" ::"s"(lds), "v"(voff), "s"(srd), "s"(soff) : "memory");
}
DEV void ald128(u32x4& d, u32x4 srd, u32 voff, u32 soff) {
  asm volatile("buffer_load_dwordx4 %0, %1, %2, %3 offen" : "=v"(d) : "v"(voff), "s"(srd), "s"(soff) : "memory");
}
DEV void ald128c(u32x4& d, u32x4 srd, u32 voff, u32 soff) {
  asm volatile("buffer_load_dwordx4 %0, %1, %2, %3 offen sc1" : "=v"(d) : "v"(voff), "s"(srd), "s"(soff) : "memory");
}
DEV void ald32c(u32& d, u32x4 srd, u32 voff, u32 soff) {
  asm volatile("buffer_load_dword %0, %1, %2, %3 offen sc1" : "=v"(d) : "v"(voff), "s"(srd), "s"(soff) : "memory");
}
DEV void st128c(u32x4 d, u32x4 srd, u32 voff, u32 soff) {
  asm volatile("buffer_store_dwordx4 %0, %1, %2, %3 offen sc1\n\ts_nop 1" ::"v"(d), "v"(voff), "s"(srd), "s"(soff) : "memory");
}
#define SB() __builtin_amdgcn_sched_barrier(0)
#define BAR() do { SB(); asm volatile("s_barrier" ::: "memory"); SB(); } while (0)
#define WAITV(n) asm volatile("s_waitcnt vmcnt(" #n ")" ::: "memory")
#define WAITL(n) asm volatile("s_waitcnt lgkmcnt(" #n ")" ::: "memory")
#define PIN(x) asm volatile("" : "+v"(x))
#define MFMA32(a, b, c) __builtin_amdgcn_mfma_f32_32x32x16_bf16(asfrag(a), asfrag(b), c, 0, 0, 0)
#define MFMA16(a, b, c) __builtin_amdgcn_mfma_f32_16x16x32_bf16(asfrag(a), asfrag(b), c, 0, 0, 0)

#define STAMP(i)
#define RSTAMP(i)
#define STAMPD(i)

DEV void bump(u32x4 ws) {
  asm volatile("s_mov_b64 exec, 1\n\tbuffer_atomic_add %0, off, %1, %2 sc1\n\ts_mov_b64 exec, -1" ::"v"(1u), "s"(ws), "s"(WS_HB) : "memory");
}
DEV void set_flag(u32x4 ws, u32 soff, u32 one) {
  asm volatile("s_mov_b64 exec, 1\n\tbuffer_store_dword %0, off, %1, %2 sc1\n\ts_mov_b64 exec, -1" ::"v"(one), "s"(ws), "s"(soff) : "memory");
}
DEV void poll_flags(u32x4 ws, u32 off, u32 v) {
  if (__builtin_expect(!__all(v == 1u), 0)) {
#pragma unroll 1
    for (int n = 0; n < SPIN_MAX; ++n) {
      if (blockIdx.x == LEADER) bump(ws);
      asm volatile("s_sleep 2\n\tbuffer_load_dword %0, %1, %2, 0 offen sc1\n\ts_waitcnt vmcnt(0)" : "=&v"(v) : "v"(off), "s"(ws) : "memory");
      if (__all(v == 1u)) break;
    }
  }
}

DEV float exp_f32(float x) {
  const float hi = x * 1.4426950408889634f, lo = __builtin_fmaf(x, 1.925963033500011e-8f, __builtin_fmaf(x, 1.4426950408889634f, -hi));
  const float e = __builtin_amdgcn_exp2f(hi);
  return __builtin_fmaf(e, lo * 0.6931471805599453f, e);
}
DEV f32x2 cexp2(float e2, float rev) {
  const float m = __builtin_amdgcn_exp2f(e2), f = __builtin_amdgcn_fractf(rev);
  f32x2 z = {m * __builtin_amdgcn_cosf(f), m * __builtin_amdgcn_sinf(f)};
  return z;
}
#define CM_MUL(t, a, z) "v_pk_mul_f32 " t ", " a ", " z " op_sel_hi:[1,0]\n\t"
#define CM_FMA(o, a, z, t) "v_pk_fma_f32 " o ", " a ", " z ", " t " op_sel:[1,1,0] op_sel_hi:[0,1,1] neg_lo:[1,0,0]\n\t"
DEV void cmul2(f32x2& p, f32x2 a, f32x2 zp, f32x2& q, f32x2 c, f32x2 zq) {
  asm(CM_MUL("%0", "%2", "%3") CM_MUL("%1", "%4", "%5") CM_FMA("%0", "%2", "%3", "%0") CM_FMA("%1", "%4", "%5", "%1")
      : "=&v"(p), "=&v"(q) : "v"(a), "v"(zp), "v"(c), "v"(zq));
}
template <int R>
DEV void tstep(u32 adr, f32x2& p, f32x2 a, f32x2 zp, f32x2& q, f32x2 c, f32x2 zq) {
  u32 t, u;
  f32x2 pn, qn;
  asm volatile("v_cvt_pk_bf16_f32 %2, %5, %6\n\tv_cvt_pk_bf16_f32 %3, %7, %8\n\t" CM_MUL("%0", "%9", "%10") CM_MUL("%1", "%11", "%12")
               CM_FMA("%0", "%9", "%10", "%0") CM_FMA("%1", "%11", "%12", "%1") "ds_write2st64_b32 %4, %2, %3 offset0:%13 offset1:%14"
               : "=&v"(pn), "=&v"(qn), "=&v"(t), "=&v"(u)
               : "v"(adr), "v"(p.x), "v"(p.y), "v"(q.x), "v"(q.y), "v"(a), "v"(zp), "v"(c), "v"(zq), "i"(17 * (R >> 3)), "i"(17 * ((R + 16) >> 3))
               : "memory");
  p = pn; q = qn;
}

__global__ __attribute__((amdgpu_waves_per_eu(2, 2))) __launch_bounds__(512) void fused_kernel(
    const void* a_x, const void* a_logA, const void* a_Aim, const void* a_Cre, const void* a_Cim, const void* a_logdt,
    const void* a_Dsk, const void* a_w1, const void* a_b1, const void* a_w2, const void* a_b2, const void* a_gw, void* a_out,
    void* a_ws) {
  u64x8 ka; u64x4 kb; u64x2 kc;
  asm volatile("s_load_dwordx16 %0, %3, 0x0\n\ts_load_dwordx8 %1, %3, 0x40\n\ts_load_dwordx4 %2, %3, 0x60"
               : "=&s"(ka), "=&s"(kb), "=&s"(kc) : "s"(__builtin_amdgcn_kernarg_segment_ptr()));
  const int tid = threadIdx.x, l = tid & 63;
  const int w = __builtin_amdgcn_readfirstlane(tid >> 6), v = w & 3;
  const int b = blockIdx.x, xcd = b & 7, r = b >> 3;
  u32 pf;
  {
    u64 pc;
    asm volatile("s_getpc_b64 %0" : "=s"(pc));
    asm volatile("global_load_dword %0, %1, %2" : "=v"(pf) : "v"(min(w * 32 + r, CODE_LINES - 1) * 128), "s"(pc & ~127ull) : "memory");
  }
  const int pl = r >> 3, cm = r & 7, nt = MAP2 ? (xcd & 1) * 4 + (r & 3) : cm;
  const bool spare = cm == 7;
  const int panel = (xcd >> 1) * 4 + pl, chunk = (xcd & 1) * 7 + (spare ? 0 : cm);
  const int gmt = xcd * 4 + pl, mt = MAP2 ? (xcd >> 1) * 8 + (r >> 2) : gmt;
  const int pair = xcd * 32 + r, dl = v >> 1;
  const int hh = l >> 5, blk = (l >> 4) & 1, q4 = (l & 15) >> 2, p4 = l & 3, l15 = l & 15, qq = l >> 4;
  const u32 po = opq(pair * 512 + (tid & 255) * 2), pdo = opq(pair * 4 + dl * 2), l16 = opq(l * 16);
  const u32 xv0 = opq((l >> 3) * 1024 + (((l & 7) ^ (l >> 4)) << 4)), xv1 = opq(xv0 ^ 64);
  const u32 w1v = opq((l >> 4) * (F * 2) + (((l & 15) ^ ((l >> 4) << 2)) << 4));
  const int ks0 = (cm + 2 * pl + (xcd >> 1)) & 7;
  const int rot = (ROTA * nt + ROTB * pl + ROTC * xcd) % 56;
  const u32 xs0 = (panel * 128 + 32 * v) * 1024, ws0 = 16 * v * (F * 2) + chunk * 256;
  const u32 v4k = opqs(v * 4096);
  u32 sx[4], sw[4];
#pragma unroll
  for (int i = 0; i < 4; ++i) { sx[i] = opqs(xs0 + ks0 * 128 + i * 7168); sw[i] = opqs(ws0 + ks0 * (64 * F * 2) + i * 13312); }
  auto dxs = [&](int s) { return opqs(ks0 + s == 7 ? -7 * 128 : 128); };
  auto dws = [&](int s) { return opqs(ks0 + s == 7 ? -7 * (64 * F * 2) : 64 * F * 2); };
  const u32 dx0 = dxs(0), dw0 = dws(0);
  asm volatile("s_waitcnt lgkmcnt(0)" : "+s"(ka), "+s"(kb), "+s"(kc) :: "memory");
  const u32x4 wsd = mksrd(kc[1], WS_END);
  if (b == LEADER) {
#pragma unroll
    for (u32 i = 0; i < 4; ++i) st128c(u32x4{0, 0, 0, 0}, wsd, l16, (w * 4 + i) * 1024);
  }
  u32 ila, iai, icr, ici, idt;
  asm volatile("global_load_ushort %4, %6, %11\n\tglobal_load_ushort %0, %5, %7\n\tglobal_load_ushort %1, %5, %8\n\t"
               "global_load_ushort %2, %5, %9\n\tglobal_load_ushort %3, %5, %10"
               : "=&v"(ila), "=&v"(iai), "=&v"(icr), "=&v"(ici), "=&v"(idt)
               : "v"(po), "v"(pdo), "s"(ka[1]), "s"(ka[2]), "s"(ka[3]), "s"(ka[4]), "s"(ka[5]) : "memory");
  const u32x4 rx = mksrd(ka[0], L * D * 2), rw1 = mksrd(ka[7], spare ? 0 : D * F * 2);
#define ISSUE(lds, va, vb, srd, so, dd)                                                                               \
  asm volatile("s_add_u32 m0, %8, %9\n\ts_nop 0\n\t"                                                                 \
               "buffer_load_dwordx4 %4, %6, %0 offen lds\n\ts_add_u32 %0, %0, %7\n\t"                                \
               "buffer_load_dwordx4 %5, %6, %1 offen offset:1024 lds\n\ts_add_u32 %1, %1, %7\n\t"                    \
               "buffer_load_dwordx4 %4, %6, %2 offen offset:2048 lds\n\ts_add_u32 %2, %2, %7\n\t"                    \
               "buffer_load_dwordx4 %5, %6, %3 offen offset:3072 lds\n\ts_add_u32 %3, %3, %7"                        \
               : "+s"(so[0]), "+s"(so[1]), "+s"(so[2]), "+s"(so[3])                                                   \
               : "v"(va), "v"(vb), "s"(srd), "s"(dd), "s"(v4k), "i"(lds) : "memory", "scc")
#define ISSUEX(s, dd) ISSUE(g1buf(s), xv0, xv1, rx, sx, dd)
#define ISSUEW(s, dd) ISSUE(g1buf(s) + 16384, w1v, w1v, rw1, sw, dd)
  const rsrc_t ws = mkrsrc(kc[1], WS_END);
  u32 hb0, bias[2];
  if (w >= 4) { ISSUEX(0, dx0); ISSUEW(0, dw0); }
  else
    asm volatile("buffer_load_dword %0, off, %4, %5 sc1\n\tglobal_load_ushort %1, %3, %6\n\tglobal_load_ushort %2, %3, %6 offset:64"
                 : "=&v"(hb0), "=&v"(bias[0]), "=&v"(bias[1])
                 : "v"((chunk * 128 + (w & 1) * 64 + (l & 31)) * 2), "s"(wsd), "s"(WS_HB), "s"(kb[0]) : "memory");
  u32 tb[8];
#pragma unroll
  for (int i = 0; i < 8; ++i) tb[i] = opq(LD_TAB + dl * 64 * TST + (tid & 127) * 4 + i * TST);
  const u32 one = opq(1);
  f32x2 za, zb, c0;
#define ANCHORS(cnt, ma, mb, sb)                                                                     \
  {                                                                                                  \
    float la, aim, cre, cim, ldt;                                                                    \
    asm volatile("s_waitcnt vmcnt(" #cnt ")\n\tv_lshlrev_b32 %4, 16, %9\n\tv_lshlrev_b32 %0, 16, %5\n\tv_lshlrev_b32 %1, 16, %6\n\t" \
                 "v_lshlrev_b32 %2, 16, %7\n\tv_lshlrev_b32 %3, 16, %8"                              \
                 : "=&v"(la), "=&v"(aim), "=&v"(cre), "=&v"(cim), "=&v"(ldt)                         \
                 : "v"(ila), "v"(iai), "v"(icr), "v"(ici), "v"(idt), "v"(pf) : "memory");            \
    if (b == LEADER) {                                 \
      BAR();                                                                                         \
      if (w == 0) { WAITV(0); bump(wsd); }                                                        \
    }                                                                                                \
    STAMP(14);                                                                                       \
    const float dt = rbf(exp_f32(ldt));                                                              \
    const float ar = -rbf(exp_f32(la)) * dt * 1.4426950408889634f;           \
    const float ai = aim * dt * 0.15915494309189535f;                       \
    za = cexp2(ma * ar, ma * ai); zb = cexp2(mb * ar, sb * ai);                                      \
    c0 = f32x2{2.f * cre * dt, 2.f * cim * dt};                                                      \
  }
#define TS(R, a, zp, c, zq) tstep<R>(tb[(R) & 7], p, a, zp, q, c, zq); if (R == EARLYW) { if (!spare) ISSUEW(1, dw1); else W2PULL(); }

  if (w < 4) {
    u32 xa[4], wa[2], ka0, ka1;
    f32x4 k0 = {0.f, 0.f, 0.f, 0.f}, k1 = k0;
    u32 cka, cua, f2o, cda, wb[3][2], da[4];
    const u32 f2w = opqs(spare ? WS_F4 + gmt * 128 + w * 4 : WS_F2 + ((panel * 2 + (w >> 1)) * 2 + (xcd & 1)) * 128 + (2 * cm + (w & 1)) * 4);
    {
      const u32 tbase = LD_TAB + dl * 64 * TST + qq * 16;
      const int e2 = p4 < 2 ? p4 : 2;
      const u32 odd = opq(w & 1);
      const u32 ra0 = opq(tbase + (odd ? 41 + 5 * (q4 >> 1) + 2 * (q4 & 1) + e2 : 16 + l15) * TST);
      const u32 ra1 = opq(tbase + (odd ? 51 + 3 * q4 + e2 : 32 + ((l15 + ((l15 >> 1) & 1)) >> 1)) * TST);
      const u32 rl = opq(tbase + l15 * TST);
      SB();
      ANCHORS(3, 1.f, 16.f, -16.f)
      f32x2 p = {1.f, 0.f}, q = c0;
#define TA(R) tstep<R>(tb[(R) & 7], p, p, zb, q, q, za);
      TA(0) TA(1) TA(2) TA(3) TA(4) TA(5) TA(6) TA(7) TA(8) TA(9) TA(10) TA(11) TA(12) TA(13) TA(14) TA(15)
      asm volatile("s_waitcnt vmcnt(0)" : "+v"(hb0), "+v"(bias[0]), "+v"(bias[1]) :: "memory");
      if (b == LEADER && w == 0) bump(wsd);
      WAITL(0);
      STAMP(1);
      BAR();
      STAMP(2);
      u32x4 fl[8], f0[8], f1[8];
      auto rdk = [&](int ks) { fl[ks] = lds128(rl + ks * 64); f0[ks] = lds128(ra0 + ks * 64); f1[ks] = lds128(ra1 + ks * 64); };
      rdk(0); SB(); rdk(1); SB(); rdk(2); SB(); rdk(3);
      SB();
#pragma unroll
      for (int ks = 0; ks < 8; ++ks) {
        if (ks + 4 < 8) rdk(ks + 4);
        SB();
        if (ks == 0) {
          const u32 xa0 = LD_B0 + (64 * (w >> 1) + (l & 31)) * 128 + ((hh ^ ((l >> 1) & 7)) << 4);
          const u32 wa0 = LD_B0 + 16384 + (8 * hh + q4) * 256 + (((8 * (w & 1) + 2 * blk + (p4 >> 1)) ^ (q4 << 2)) << 4) + 8 * (p4 & 1);
#pragma unroll
          for (int s = 0; s < 4; ++s) xa[s] = opq(xa0 ^ (s << 5));
          wa[0] = opq(wa0); wa[1] = opq(wa0 ^ 64);
        }
        if (ks == 3) {
          const u32 kb0 = LD_K + dl * 2 * KST - 32 * l15;
          ka0 = opq(kb0 + (odd ? 2 * (2040 - 512 - 256 * (qq >> 1) - 8 * (qq & 1)) : 2 * (2044 - 256 - 4 * qq)));
          ka1 = opq(kb0 + 2 * (2032 - 1024 - 256 * qq));
          cka = opq(LD_K + dl * 2 * KST + ((l & 1) ? 2 * (30 - 2 * l15) : KST + 2 * (32 - 2 * l15)) + 16 * qq + 256 * (w & 1));
          cua = opq(LD_U + dl * 2 * UST + ((l & 1) ? 0 : UST) + 64 * (l15 >> 1) + 16 * qq + 256 * (w & 1));
        }
        SB();
        k0 = MFMA16(f0[ks], fl[ks], k0);
        k1 = MFMA16(f1[ks], fl[ks], k1);
        SB();
      }
    }
    auto kwrite = [&]() {
      if (w & 1) {
        const u32 a2 = pk(k0[2], k0[2]), a1 = pk(k0[1], k0[1]), am = pk(k0[1], k0[0]), a0 = pk(k0[0], k0[0]);
        sts128(ka0, u32x4{a2, a1, am, a0});
        sts128(ka0 + KST, u32x4{a2, a2, a1, am});
        const u32 c2 = pk(k1[2], k1[2]), c1 = pk(k1[1], k1[1]), cm = pk(k1[1], k1[0]), c0 = pk(k1[0], k1[0]);
        sts128(ka1, u32x4{c2, c2, c1, c1}); sts128(ka1 + 16, u32x4{c1, cm, c0, c0});
        sts128(ka1 + KST, u32x4{c2, c2, c2, c1}); sts128(ka1 + KST + 16, u32x4{c1, c1, cm, c0});
      } else {
        const u32 a0 = pk(k0[3], k0[2]), a1 = pk(k0[1], k0[0]), c0 = pk(k1[3], k1[2]), c1 = pk(k1[1], k1[0]), c3 = pk(k1[3], k1[3]);
        sts64(ka0 + 512, u32x2{a0, a1}); sts32(ka0 + 512 + KST + 4, a0); sts32(ka0 + 512 + KST + 8, a1);
        sts64(ka0, u32x2{c0, c1}); sts64(ka0 + KST, u32x2{c3, c0}); sts32(ka0 + KST + 8, c1);
        sts32(LD_K + dl * 2 * KST + 4096 + blk * (KST + 4) + l15 * 4, 0);
      }
    };
    auto addr2 = [&](int part) {
      if (part == 0) f2o = opq(WS_F2 + (mt * 2 + (w >> 1)) * 128 + (l < 14 ? l : 13) * 4);
      if (part == 1) cda = opq(((l15 >> 1) * 4 + qq) ^ ((l & 1) | ((l15 >> 1) & 2)));
      if (part == 2) cda = opq(LD_DUMP + (w & 1) * 16384 + dl * 2048 + ((l & 1) * 32 + cda) * 16);
      if (part == 3) wb[0][0] = opq((((2 * blk + (p4 >> 1)) ^ ((q4 & 2) << 1)) << 4) + 8 * (p4 & 1));
      if (part == 4) { wb[0][0] = opq(wb[0][0] + (w >> 1) * 2048 + (4 * hh + q4) * 128); wb[0][1] = opq(wb[0][0] ^ 64); }
      if (part == 5) { for (int i = 1; i < 3; ++i) { wb[i][0] = opq(wb[0][0] + i * 65536); wb[i][1] = opq(wb[0][1] + i * 65536); } }
      if (part == 6) da[0] = opq(LD_DUMP + (w >> 1) * 16384 + (32 * (w & 1) + (l & 31)) * 256 + ((hh ^ (l & 7)) << 4));
      if (part == 7) { for (int g = 1; g < 4; ++g) da[g] = opq(da[0] ^ (g << 5)); }
    };
    const u32 f1w = opqs(w ? WS_END : WS_F1 + ((2 * ks0 + (xcd & 1)) * 4 + (xcd >> 1)) * 128 + pl * 4);
    hb0 = opq(w ? 1u : hb0);
    auto hb_check = [&](u32 hv) {
      if (__builtin_expect(!__all(hv != hb0), 0)) {
#pragma unroll 1
        for (int n = 0; n < SPIN_MAX; ++n) {
          asm volatile("s_sleep 4\n\tbuffer_load_dword %0, off, %1, %2 sc1\n\ts_waitcnt vmcnt(0)" : "=&v"(hv) : "s"(wsd), "s"(WS_HB) : "memory");
          if (__all(hv != hb0)) break;
        }
      }
      set_flag(wsd, f1w, one);
    };
    u32 hb2;
    asm volatile("buffer_load_dword %0, off, %1, %2 sc1" : "=v"(hb2) : "s"(wsd), "s"(w ? WS_END : WS_HB) : "memory");
    STAMP(3); RSTAMP(26);
    if (!spare) {
      const int tm = w >> 1, fh = w & 1;
      f32x16 acc[2][2];
      u32x4 fa[2][2], fb[2][2];
#pragma unroll
      for (int i = 0; i < 2; ++i) {
        fa[1][i] = u32x4{hh ? 0 : bias[i], 0, 0, 0}; fb[1][i] = u32x4{0x3f80, 0, 0, 0};
#pragma unroll
        for (int j = 0; j < 16; ++j) { acc[i][0][j] = 0.f; acc[i][1][j] = 0.f; }
      }
      if (KWAT == -1) kwrite();
      const u32 hb = opqs(WS_H + ((panel * 2 + tm) * 2 * 112 + 8 * chunk + 4 * fh) * 1024);
      WAITL(0);
#define RDA(n, s) fb[n][0] = lds128(xa[s]); fa[n][0] = cat(ldstr(wa[0] + (s) * 4096), ldstr(wa[0] + (s) * 4096 + 1024)); SB();
#define RDB(n, s) fb[n][1] = lds128(xa[s] + 4096); fa[n][1] = cat(ldstr(wa[1] + (s) * 4096), ldstr(wa[1] + (s) * 4096 + 1024)); SB();
#define MM(n, i, j) acc[i][j] = MFMA32(fa[n][i], fb[n][j], acc[i][j]); SB();
#define ADV(a) if (s < 7) { a += dlt; PIN(a); }
#pragma unroll
      for (int s = 0; s < 8; ++s) {
        const u32 dlt = g1buf(s + 1) - g1buf(s);
        BAR();
        if (s) { MM(0, 1, 1) }
        if (s == 7) { STAMP(25); }
        RDA(0, 0) MM(1, 0, 0) RDB(0, 0) MM(1, 0, 1)
        if (s == 2) { u32 hv; asm volatile("s_waitcnt vmcnt(0)\n\tv_mov_b32 %0, %1" : "=v"(hv) : "v"(hb2) : "memory"); hb_check(hv); }
        SB();
        MM(1, 1, 0) ADV(xa[0]) SB();
        MM(1, 1, 1) RDA(1, 1) MM(0, 0, 0) RDB(1, 1) MM(0, 0, 1) ADV(xa[1]) SB(); MM(0, 1, 0) if (s >= 3 && s < 7) { addr2(2 * s - 6); SB(); }
        MM(0, 1, 1) RDA(0, 2) MM(1, 0, 0) RDB(0, 2) MM(1, 0, 1) ADV(xa[2]) SB(); MM(1, 1, 0) if (s >= 3 && s < 7) { addr2(2 * s - 5); SB(); }
        MM(1, 1, 1) RDA(1, 3) MM(0, 0, 0) RDB(1, 3)
        if (s < 7) { MM(0, 0, 1) ADV(xa[3]) ADV(wa[0]) ADV(wa[1]) SB(); MM(0, 1, 0) }
        if (s == KWAT) { kwrite(); SB(); }
      }
      u32x4 ho;
      auto act = [&](int ft, int tt, int j) {
        f32x2 rr;
        rr.x = __int_as_float(max(__float_as_int(acc[ft][tt][2 * j]), 0));
        rr.y = __int_as_float(max(__float_as_int(acc[ft][tt][2 * j + 1]), 0));
        rr = rr * rr;
        ho[j & 3] = pk(rr.x, rr.y);
        if ((j & 3) == 3) st128c(ho, wsd, l16, hb + (tt * 112 + 2 * ft + (j >> 2)) * 1024);
        SB();
      };
      MM(0, 1, 0) MM(1, 0, 0) MM(1, 1, 0) MM(0, 0, 1)
      STAMP(4);
      act(0, 0, 0); act(0, 0, 1); MM(1, 0, 1) act(0, 0, 2); act(0, 0, 3); MM(0, 1, 1) act(0, 0, 4); act(0, 0, 5); MM(1, 1, 1) act(0, 0, 6); act(0, 0, 7);
#pragma unroll
      for (int j = 0; j < 8; ++j) act(1, 0, j);
#pragma unroll
      for (int j = 0; j < 8; ++j) act(0, 1, j);
#pragma unroll
      for (int j = 0; j < 8; ++j) act(1, 1, j);
      if (KWAT == 8) { kwrite(); WAITL(0); }
    } else {
      u32x4 gwv, xr[16];
      asm volatile("global_load_dwordx4 %0, %1, %2" : "=v"(gwv) : "v"(l16), "s"(kb[3]) : "memory");
#pragma unroll
      for (int i = 0; i < 16; ++i) ald128(xr[i], rx, l16, (gmt * 64 + w * 16 + i) * 1024);
#pragma unroll
      for (int i = 0; i < 8; ++i) addr2(i);
      kwrite();
      BAR();
      u32 hv;
      asm volatile("s_waitcnt vmcnt(0)\n\tv_mov_b32 %0, %1" : "=v"(hv) : "v"(hb2) : "memory");
      PIN(gwv);
#pragma unroll
      for (int i = 0; i < 16; ++i) PIN(xr[i]);
      hb_check(hv);
      BAR();
      f32x4 s4 = {0.f, 0.f, 0.f, 0.f};
#pragma unroll 1
      for (int h2 = 0; h2 < 2; ++h2) {
        if (b == LEADER && w == 0) bump(wsd);
#pragma unroll
        for (int i = 0; i < 8; ++i) {
          u32x4 pr;
#pragma unroll
          for (int j = 0; j < 4; ++j) pr[j] = pk(bflo(xr[i][j]) * bflo(gwv[j]), bfhi(xr[i][j]) * bfhi(gwv[j]));
          const u32 sel = l15 == h2 * 8 + i ? 0x3f803f80u : 0u;
          s4 = MFMA16((u32x4{sel, sel, sel, sel}), pr, s4);
          xr[i] = xr[i + 8];
        }
      }
      u32x4 g4;
#pragma unroll
      for (int e = 0; e < 4; ++e) {
        float t = s4[e];
        t += __int_as_float(__builtin_amdgcn_update_dpp(0, __float_as_int(t), 0x128, 0xf, 0xf, true));
        t += __int_as_float(__builtin_amdgcn_update_dpp(0, __float_as_int(t), 0x124, 0xf, 0xf, true));
        t += __int_as_float(__builtin_amdgcn_update_dpp(0, __float_as_int(t), 0x122, 0xf, 0xf, true));
        t += __int_as_float(__builtin_amdgcn_update_dpp(0, __float_as_int(t), 0x121, 0xf, 0xf, true));
        g4[e] = __float_as_uint(rbf(__builtin_amdgcn_rcpf(1.f + exp_f32(-rbf(t)))));
      }
      if (l15 == 0) st128c(g4, wsd, qq * 16, WS_G + (gmt * 64 + w * 16) * 4);
      WAITL(0);
      STAMP(4);
    }
    STAMP(5);
    BAR();
    STAMP(6); RSTAMP(27);
    const int kh = w >> 1, th = w & 1;
    u32 f2v;
    {
      f32x4 y[8];
      u32x2 kl[2][8], kr[2][8];
      u32x4 uf[2][8];
      auto rdc = [&](int i, int j, int part) {
        if (part == 0) uf[i & 1][j] = lds128(cua + 64 * i + 512 * j);
        if (part == 1) kl[i & 1][j] = lds64v(cka + 64 * i + 512 * (7 - j));
        if (part == 2) kr[i & 1][j] = lds64v(cka + 64 * i + 512 * (7 - j) + 8);
      };
#pragma unroll
      for (int j = 0; j < 12; ++j) rdc(0, j / 3, j % 3);
      SB();
#pragma unroll
      for (int i = 0; i < 4; ++i) {
        int cnt = 0;
#pragma unroll
        for (int m = 0; m < 8; ++m) {
#pragma unroll
          for (int k = 0; k <= 2 * m; ++k) {
            const int de = k <= m ? k : m, si = k <= m ? m : 2 * m - k, t = de + si;
            if (t <= 7) {
              const u32x4 kf = cat(kl[i & 1][de], kr[i & 1][de]);
              if (i == 0 && si == (t + 1) / 2) asm volatile("v_mfma_f32_16x16x32_bf16 %0, %1, %2, 0" : "=&v"(y[t]) : "v"(kf), "v"(uf[0][si]));
              else asm volatile("v_mfma_f32_16x16x32_bf16 %0, %1, %2, %0" : "+v"(y[t]) : "v"(kf), "v"(uf[i & 1][si]));
              SB();
              if (cnt < 12) { rdc(i, 4 + cnt / 3, cnt % 3); SB(); }
              if (i < 3 && cnt >= 16 && cnt < 28) { rdc(i + 1, (cnt - 16) / 3, (cnt - 16) % 3); SB(); }
              if (i * 36 + cnt == FLAGC) { STAMP(22); WAITV(0); STAMP(23); RSTAMP(29); set_flag(wsd, f2w, one); SB(); }
              ++cnt;
            }
          }
        }
      }
      STAMP(7);
#pragma unroll
      for (int t = 0; t < 8; ++t) {
        if (t == POLLC) { RSTAMP(30); ald32c(f2v, wsd, f2o, 0); }
        if (t == 6) asm volatile("s_nop 7" : "+v"(y[6]), "+v"(y[7]));
        sts128(cda + (t & 3) * 4096 + (t >> 2) * 1024, __builtin_bit_cast(u32x4, y[t]));
        SB();
      }
    }
    {
      const u32 hs0 = opqs(WS_H + ((mt * 2 + th) * 112 + 56 * kh) * 1024), hs1 = opqs(hs0 + 56 * 1024);
      u32 hs = opqs(hs0 + rot * 1024);
      WAITL(0);
      STAMP(8);
      BAR();
      STAMP(13);
      u32x4 hf[56], a0[56], a1[56];
      auto ldh = [&](int s) {
        hf[s] = __builtin_amdgcn_raw_buffer_load_b128(ws, l16, hs, SC1);
        hs += 1024;
        if (ROTA | ROTB | ROTC) hs = hs == hs1 ? hs0 : hs;
      };
      auto rdw = [&](int s, int dt) {
        const u32 a = wb[slot(s) >> 16][dt] + (slot(s) & 65535);
        return cat(ldstr(a), ldstr(a + 1024));
      };
#pragma unroll
      for (int s = 0; s < NPA; ++s) { a0[s] = rdw(s, 0); a1[s] = rdw(s, 1); SB(); }
      asm volatile("s_waitcnt vmcnt(0)" : "+v"(f2v) :: "memory");
      STAMP(24);
      poll_flags(wsd, f2o, f2v);
      STAMP(9); RSTAMP(31);
#pragma unroll
      for (int s = 0; s < NH0; ++s) ldh(s);
      SB();
#pragma unroll
      for (int s = NPA; s < NPR; ++s) { a0[s] = rdw(s, 0); a1[s] = rdw(s, 1); SB(); }
      f32x16 o[2];
#pragma unroll
      for (int i = 0; i < 16; ++i) { o[0][i] = 0.f; o[1][i] = 0.f; }
      WAITL(0);
      STAMP(10); RSTAMP(28);
      BAR();
#pragma unroll
      for (int s = NH0; s < HRING; ++s) ldh(s);
      SB();
#pragma unroll
      for (int s = 0; s < 56; ++s) {
        if (s + 2 == X1 || s + 2 == X2) { STAMPD(18 + (s + 2 == X2)); WAITL(0); BAR(); STAMPD(20 + (s + 2 == X2)); }
        if (s + 2 < 56 && s + 2 >= NPR) { a0[s + 2] = rdw(s + 2, 0); SB(); }
        o[0] = MFMA32(a0[s], hf[s], o[0]); SB();
        if (s + 2 < 56 && s + 2 >= NPR) { a1[s + 2] = rdw(s + 2, 1); SB(); }
        o[1] = MFMA32(a1[s], hf[s], o[1]); SB();
        if (s + HRING < 56) { ldh(s + HRING); SB(); }
      }
      STAMP(11);
#pragma unroll
      for (int dt = 0; dt < 2; ++dt)
#pragma unroll
        for (int g = 0; g < 4; ++g)
          sts128(da[g] + dt * 128, __builtin_bit_cast(u32x4, f32x4{o[dt][4 * g], o[dt][4 * g + 1], o[dt][4 * g + 2], o[dt][4 * g + 3]}));
      WAITL(0);
    }
    BAR();
    STAMP(12);
    if (b == LEADER && w == 0) {
#pragma unroll 1
      for (int n = 0; n < SPIN_MAX; ++n) {
        u32x4 a;
        asm volatile("buffer_load_dwordx4 %0, %1, %2, %3 offen sc1\n\ts_waitcnt vmcnt(0)" : "=&v"(a) : "v"(l * 128), "s"(wsd), "s"(WS_F1) : "memory");
        if (__all((a.x & a.y & a.z & a.w) == 1u)) break;
        bump(wsd);
        __builtin_amdgcn_s_sleep(8);
      }
    }
  } else {
    __builtin_amdgcn_s_setprio(1);
    u32 pf2 = opq(0);
    {
      const int rw = 8 * (p4 & 1) + q4;
      const u32 ta0 = LD_B0 + (32 * v + rw) * 128 + (((4 * (xcd & 1) + qq) ^ (rw >> 1)) << 4) + 8 * (p4 >> 1);
      const u32 ta = opq(ta0), tb2 = opq((ta0 ^ 32) + 512);
      const u32 uv = opq((l >> 3) * 16384 + (l & 7) * 16);
      const u32 us = opqs(WS_UT + (16 * ks0 + 8 * (xcd & 1)) * 16384 + (panel * 128 + 32 * v) * 8);
      const u32 dx1 = dxs(1), dw1 = dws(1);
      const u32 pv = opq(((xcd * 16 + pl * 4 + v) * 112 + (l < 28 ? l : 27)) * 128);
#define W2PULL() asm volatile("global_load_dword %0, %1, %2\n\tglobal_load_dword %0, %1, %2 offset:3584\n\t"   \
                              "global_load_dword %0, %1, %3\n\tglobal_load_dword %0, %1, %3 offset:3584" : "+v"(pf2) : "v"(pv), "s"(kb[1]), "s"(kb[1] + 7168) : "memory")
      SB();
      ANCHORS(8, 2.f, 256.f, 256.f)
      const f32x2 z2 = za, z256 = zb;
      f32x2 z4, z8, z512, z768, c3, p, q, cp, cq;
      cmul2(z4, z2, z2, z512, z256, z256);
      cmul2(z8, z4, z4, z768, z512, z256);
      cmul2(p, c0, z256, c3, c0, z768);
      cmul2(q, c3, z8, cq, c3, z8);
      cp = p;
      TS(32, p, z2, q, z4) TS(33, p, z2, q, z4) TS(34, p, z2, c3, z256) cq = q;
      TS(35, p, z2, q, z8) TS(36, p, z2, q, z8) TS(37, p, z2, cq, z256) cq = q;
      TS(38, p, z2, q, z8) TS(39, p, z2, q, z8) TS(40, cp, z256, cq, z256) cq = q; cp = p;
      TS(41, p, z4, q, z8) TS(42, p, z4, q, z8) TS(43, p, z4, cq, z256)
      TS(44, p, z4, q, z8) TS(45, cp, z256, q, z8) TS(46, p, z4, q, z8) TS(47, p, z4, q, z8)
      WAITL(0);
      BAR();
      WAITV(8);
      const u32x4 t0 = cat(ldstr(ta), ldstr(tb2)), t1 = cat(ldstr(ta + 2048), ldstr(tb2 + 2048));
      st128c(t0, wsd, uv, us);
      st128c(t1, wsd, uv, us + 128);
      if (!spare) ISSUEX(1, dx1);
    }
    __builtin_amdgcn_s_setprio(0);
    STAMP(1);
    const u32x4 rw2 = mksrd(kb[1], F * D * 2);
    const u32 f1o = WS_F1 + ((2 * xcd + (r >> 4)) * 4 + v) * 128 + p4 * 4;
    if (!spare) {
      u32 dx = dxs(2);
#define G1LD(s) BAR(); ISSUEX(s, dx); STAMPD(16 + s); { const u32 dw = dws(s); dx = dxs(s + 1); ISSUEW(s, dw); }
      WAITV(10); G1LD(2)
      WAITV(8); G1LD(3) WAITV(8); G1LD(4) WAITV(8); G1LD(5) WAITV(8); G1LD(6) WAITV(8); G1LD(7)
      WAITV(8);
    } else {
      WAITV(0);
      BAR();
      BAR();
#pragma unroll 1
      for (int j = 0; j < SLPF; ++j)
        asm volatile("global_load_dword %0, %1, %2" : "+v"(pf2)
                     : "v"((j * 896 + (pl * 4 + v) * 56 + (l < 56 ? l : 55)) * 1024 + ((3 - ((u32)kb[1] >> 7)) & 7) * 128), "s"(kb[1]) : "memory");
      asm volatile("s_waitcnt vmcnt(1)" ::: "memory");
    }
    const u32 w2v = (l >> 3) * 1024 + (((l & 7) ^ (((l >> 4) & 1) << 2)) << 4);
    const u32 w2s = (896 * (v >> 1) + 8 * (v & 1)) * 1024 + nt * 128;
    constexpr int NR = 56 - NDMA;
    u32x4 wr[NR];
    auto w2o = [&](int step) { return w2s + (step + rot >= 56 ? step + rot - 56 : step + rot) * 16384; };
    auto ldw2 = [&](int step) { glds(rw2, slot(step) + v * 1024, w2v, w2o(step)); };
    auto ldw2r = [&](int i) { ald128(wr[i], rw2, w2v, w2o(NDMA + i)); };
    u32 f1v;
    STAMP(3);
    if (!spare) BAR();
    ald32c(f1v, wsd, f1o, 0);
#pragma unroll
    for (int i = 0; i < NP7; ++i) ldw2r(i);
    if (!spare) { asm volatile("s_waitcnt vmcnt(%0)" ::"i"(NP7 + 1) : "memory"); BAR(); }
#pragma unroll
    for (int i = NP7; i < 8; ++i) ldw2r(i);
    asm volatile("s_waitcnt vmcnt(8)" : "+v"(f1v), "+v"(pf2) :: "memory");
    const int d = pair * 2 + dl;
    const int c8 = l & 7, tr = 8 * v + (l >> 3);
    u32 dskr;
    u32x4 b2v;
    poll_flags(wsd, f1o, f1v);
    STAMP(4);
    {
      u32x4 uu[2];
#pragma unroll
      for (int k = 0; k < 2; ++k)
        ald128c(uu[k], wsd, (l >> 1) * 64 + (l & 1) * 16, WS_UT + (pair >> 1) * 16384 + (64 * v + 32 * k) * 64 + (pair & 1) * 32);
      asm volatile("global_load_ushort %0, %1, %2" : "=v"(dskr) : "v"(pdo), "s"(ka[6]) : "memory");
      asm volatile("global_load_dwordx4 %0, %1, %2" : "=v"(b2v) : "v"(nt * 128 + c8 * 16), "s"(kb[2]) : "memory");
#pragma unroll
      for (int i = 8; i < 8 + NPRE; ++i) ldw2r(i);
      if (l < 28) sts128(LD_U + v * UST + l16, u32x4{0, 0, 0, 0});
      if (l == 28 && (v & 1)) sts16(LD_U + v * UST + 448, 0);
      const u32 a0 = opq(LD_U + (l & 1) * 2 * UST + 448 + (64 * v + (l >> 1)) * 16);
      asm volatile("s_waitcnt vmcnt(%2)" : "+v"(uu[0]), "+v"(uu[1]) : "i"(NPRE + 2) : "memory");
#pragma unroll
      for (int k = 0; k < 2; ++k) {
        const u32 a = a0 + 512 * k;
        sts128(a, uu[k]);
        sts16(a + UST + 2, uu[k].x);
        sts32(a + UST + 4, __builtin_amdgcn_alignbyte(uu[k].y, uu[k].x, 2));
        sts64(a + UST + 8, u32x2{__builtin_amdgcn_alignbyte(uu[k].z, uu[k].y, 2), __builtin_amdgcn_alignbyte(uu[k].w, uu[k].z, 2)});
        sts16(a + UST + 16, uu[k].w >> 16);
      }
    }
    u32 wsa[3];
#pragma unroll
    for (int i = 0; i < 3; ++i) wsa[i] = opq(i * 65536 + v * 1024 + l16);
    auto stw2 = [&](int i) { PIN(wr[i]); sts128(wsa[slot(NDMA + i) >> 16] + (slot(NDMA + i) & 65535), wr[i]); };
    const int a5 = l & 31;
    const u32 dpe = opq(LD_DUMP + v * 1024 + hh * 4096 + (a5 ^ ((a5 >> 2) & 2)) * 16), dpo = opq((dpe ^ 16) + 512);
    const u32 ats = opqs(WS_AT + d * 4096 + (v & 1) * 2048);
    const u32 f3a = opq(WS_F3 + ((mt >> 4) * 32 + nt * 4 + v) * 128 + l15 * 4), f3b = opq(WS_F4 + mt * 128 + p4 * 4);
    const u32 f3o = opq(l < 16 ? f3a : f3b);
    const u32 avv = opq((l >> 3) * 4096 + c8 * 16), gv = opq(tr * 4);
    const u32 f3w = opqs(WS_F3 + ((v & 1) * 32 + (d >> 4)) * 128 + (d & 15) * 4);
    u32 avs[2], gs[2];
#pragma unroll
    for (int k = 0; k < 2; ++k) { avs[k] = opqs(WS_AT + (nt * 64 + 16 * v + 8 * k) * 4096 + mt * 128); gs[k] = opqs(WS_G + mt * 256 + k * 128); }
    const u32 uka = opq(LD_U + dl * 2 * UST + 448 + 2048 * (v & 1) + l16);
    WAITL(0);
    STAMP(5);
    BAR();
    u32x4 uk[2];
#pragma unroll
    for (int k = 0; k < 2; ++k) uk[k] = lds128(uka + 1024 * k);
    SB();
#pragma unroll
    for (int s = 0; s < NDMA; ++s) ldw2(s);
#pragma unroll
    for (int i = 8 + NPRE; i < NR - NLATE; ++i) ldw2r(i);
    asm volatile("s_waitcnt vmcnt(%2)" : "+v"(dskr), "+v"(b2v) : "i"(NR - NLATE - 8 - NPRE) : "memory");
    WAITL(0);
    __builtin_amdgcn_s_setprio(1);
    STAMP(6);
    BAR();
    STAMP(7);
    {
      f32x4 e[2][2], o[2][2];
#pragma unroll
      for (int k = 0; k < 2; ++k)
#pragma unroll
        for (int h2 = 0; h2 < 2; ++h2) {
          e[k][h2] = __builtin_bit_cast(f32x4, lds128(dpe + k * 8192 + h2 * 16384));
          o[k][h2] = __builtin_bit_cast(f32x4, lds128(dpo + k * 8192 + h2 * 16384));
        }
      SB();
      const float dsk = bf2f(dskr);
#pragma unroll
      for (int k = 0; k < 2; ++k) {
        const f32x4 ee = e[k][0] + e[k][1], oo = o[k][0] + o[k][1];
        u32x4 res;
#pragma unroll
        for (int j = 0; j < 4; ++j) res[j] = pk(__builtin_fmaf(bflo(uk[k][j]), dsk, ee[j]), __builtin_fmaf(bfhi(uk[k][j]), dsk, oo[j]));
        st128c(res, wsd, l16, ats + k * 1024);
      }
    }
#pragma unroll
    for (int i = NR - NLATE; i < NR; ++i) ldw2r(i);
    __builtin_amdgcn_s_setprio(WPRIO);
    static_assert(NW0 <= 40 - NDMA && F3AT <= NR - NLATE, "slots of steps >= 40 are free after X0; late loads are covered by the wait at F3AT");
#pragma unroll
    for (int i = 0; i < 40 + NPR - NDMA; ++i) {
      if (i == NW0) { STAMP(8); BAR(); }
      if (i == 8 + NPRE) asm volatile("s_waitcnt vmcnt(%0)" ::"i"(NLATE + 2) : "memory");
      if (i == F3AT) { WAITV(0); __builtin_amdgcn_s_setprio(1); set_flag(wsd, f3w, one); SB(); __builtin_amdgcn_s_setprio(WPRIO); }
      stw2(i);
    }
    WAITL(0);
    __builtin_amdgcn_s_setprio(1);
    STAMP(9);
    BAR();
    u32 f3v, gr[2];
    u32x4 av[2];
    ald32c(f3v, wsd, f3o, 0);
#pragma unroll
    for (int i = 40 + NPR - NDMA; i < NR; ++i) stw2(i);
    asm volatile("s_waitcnt vmcnt(0)" : "+v"(f3v) :: "memory");
    poll_flags(wsd, f3o, f3v);
#pragma unroll
    for (int k = 0; k < 2; ++k) ald128c(av[k], wsd, avv, avs[k]);
#pragma unroll
    for (int k = 0; k < 2; ++k) ald32c(gr[k], wsd, gv, gs[k]);
    {
      const u32 a = opq(LD_AST + (16 * v + (l >> 3)) * 132 + c8 * 16);
      asm volatile("s_waitcnt vmcnt(0)" : "+v"(av[0]), "+v"(av[1]), "+v"(gr[0]), "+v"(gr[1]) :: "memory");
#pragma unroll
      for (int k = 0; k < 2; ++k) {
        sts32(a + k * 1056, av[k].x); sts32(a + k * 1056 + 4, av[k].y); sts32(a + k * 1056 + 8, av[k].z); sts32(a + k * 1056 + 12, av[k].w);
      }
    }
    const u32 aa = opq(LD_AST + c8 * 8 * 132 + tr * 2);
    WAITL(0);
    STAMP(10);
    BAR();
    f32x2 Q[2][4], og[2];
    {
      u32 ar[2][8];
#pragma unroll
      for (int k = 0; k < 2; ++k)
#pragma unroll
        for (int j = 0; j < 8; ++j) ar[k][j] = *(LAS u16*)(size_t)(aa + j * 132 + 64 * k);
      SB();
#pragma unroll
      for (int k = 0; k < 2; ++k) {
        const float g = __uint_as_float(gr[k]), o1 = rbf(1.f - g);
        og[k] = f32x2{o1, o1};
#pragma unroll
        for (int j = 0; j < 4; ++j) {
          Q[k][j] = f32x2{g * bf2f(ar[k][2 * j]) + o1 * bflo(b2v[j]), g * bf2f(ar[k][2 * j + 1]) + o1 * bfhi(b2v[j])};
          PIN(Q[k][j]);
        }
      }
    }
    const u32 d0 = opq(LD_DUMP + tr * 256 + (((2 * c8) ^ (tr & 7)) << 4)), d1 = opq(d0 ^ 16);
    const u32 oo0 = opq((mt * 64 + tr) * (D * 2) + nt * 128 + c8 * 16), oo1 = opq(oo0 + 32 * D * 2);
    STAMP(11);
    BAR();
    STAMP(12);
    f32x4 pe[2][2], po2[2][2];
#pragma unroll
    for (int k = 0; k < 2; ++k) {
#pragma unroll
      for (int h2 = 0; h2 < 2; ++h2) {
        pe[k][h2] = __builtin_bit_cast(f32x4, lds128(d0 + k * 8192 + h2 * 16384));
        po2[k][h2] = __builtin_bit_cast(f32x4, lds128(d1 + k * 8192 + h2 * 16384));
      }
      SB();
    }
#pragma unroll
    for (int k = 0; k < 2; ++k) {
      const f32x4 e = pe[k][0] + pe[k][1], o = po2[k][0] + po2[k][1];
      const f32x2 r0 = og[k] * e.xy + Q[k][0], r1 = og[k] * e.zw + Q[k][1], r2 = og[k] * o.xy + Q[k][2], r3 = og[k] * o.zw + Q[k][3];
      *(GAS u32x4*)(kc[0] + (k ? oo1 : oo0)) = u32x4{pk(r0.x, r0.y), pk(r1.x, r1.y), pk(r2.x, r2.y), pk(r3.x, r3.y)};
      SB();
    }
    STAMP(13);
  }
}

extern "C" void kernel_launch(void* const* d_in, const int* in_sizes, int n_in, void* d_out, int out_size, void* d_ws,
                              size_t ws_size, hipStream_t stream) {
  (void)hipFuncSetAttribute((const void*)fused_kernel, hipFuncAttributeMaxDynamicSharedMemorySize, LDS_BYTES);
  fused_kernel<<<256, 512, LDS_BYTES, stream>>>(d_in[0], d_in[1], d_in[2], d_in[3], d_in[4], d_in[5], d_in[6], d_in[8], d_in[9],
                                                d_in[10], d_in[11], d_in[12], d_out, d_ws);
}
```

```cpp
#include <hip/hip_runtime.h>
#include <utility>

typedef unsigned u32;
typedef unsigned short u16;
typedef unsigned long long u64;
typedef u32 u32x2 __attribute__((ext_vector_type(2)));
typedef u32 u32x4 __attribute__((ext_vector_type(4)));
typedef short s16x4 __attribute__((ext_vector_type(4)));
typedef __bf16 bf16x8 __attribute__((ext_vector_type(8)));
typedef __bf16 bf16x2 __attribute__((ext_vector_type(2)));
typedef float f32x2 __attribute__((ext_vector_type(2)));
typedef float f32x4 __attribute__((ext_vector_type(4)));
typedef u64 u64x2 __attribute__((ext_vector_type(2)));
typedef u64 u64x4 __attribute__((ext_vector_type(4)));
typedef u64 u64x8 __attribute__((ext_vector_type(8)));
#define GAS __attribute__((address_space(1)))
#define LAS __attribute__((address_space(3)))
#define DEV __device__ __forceinline__

constexpr int L = 2048, D = 512, F = 1792;
constexpr int SC1 = 16;
constexpr int CODE_LINES = 216;

constexpr int EARLYW = 40;
constexpr int HBL = 1;
constexpr int SPW = 12;
constexpr int PULL0 = 32;
constexpr int PULL1 = 42;
constexpr int NTL = 2;
constexpr int DL = 8;
constexpr int HRING = 12;
constexpr int NP7 = 4;
constexpr int NFL = 6;
constexpr int X0V = 22;
constexpr int X1V = 30;
constexpr int X2V = 52;
constexpr int G8AT = 6;
constexpr int EARLYX = 46;
constexpr int NPO = 3;
constexpr int HBC = 4;

constexpr u32 WS_F2 = 0;
constexpr u32 WS_F3 = 8192;
constexpr u32 WS_F1 = 16384;
constexpr u32 WS_F4 = 24576;
constexpr u32 WS_HB = 32768;
constexpr u32 WS_G = 36864;
constexpr u32 WS_UT = 65536;
constexpr u32 WS_AT = WS_UT + 2097152;
constexpr u32 WS_H = WS_AT + 2097152;
constexpr u32 WS_END = WS_H + 7340032;
constexpr int LEADER = 56;
constexpr int SPIN_MAX = 1 << 16;

constexpr u32 KST = 4224, UST = 4576, TST = 544, CHS = 2 * KST + 2 * UST;
constexpr u32 LD_TAB = 0;
constexpr u32 LD_B0 = 98304;
constexpr u32 LD_CH = 98304;
constexpr u32 LD_SYNC = LD_CH + 2 * CHS;
constexpr u32 LD_AST = 8192;
constexpr u32 LD_FD = 32768;
constexpr u32 LDS_BYTES = 163840;
constexpr int NDMA = 48;
constexpr int X0 = X0V, X1 = X1V, X2 = X2V;
static_assert(X0 >= 20 && X1 <= 30 && !((X0 | X1 | X2) & 1) && X2 >= 39 && X2 <= 56 - 2 * NTL && HRING >= 2 * NTL, "slot reuse");
constexpr u32 g1buf(int s) { return s == 0 ? 98304u : s == 4 ? 32768u : (s & 3) == 1 ? 131072u : (s & 3) == 2 ? 65536u : 0u; }
constexpr u32 slot(int s) { return s >= 31 ? slot(s - 31) : 4096u * (s < 8 ? s + 8 : s < 15 ? s + 25 : s < 23 ? s - 15 : s - 7); }

typedef __amdgpu_buffer_rsrc_t rsrc_t;
DEV rsrc_t mkrsrc(u64 p, u32 bytes) { return __builtin_amdgcn_make_buffer_rsrc((void*)p, 0, bytes, 0x00020000); }
DEV u32x4 mksrd(u64 a, u32 bytes) { return u32x4{(u32)a, (u32)(a >> 32) & 0xffffu, bytes, 0x00020000u}; }
DEV float bf2f(u32 x) { return __uint_as_float(x << 16); }
DEV float bflo(u32 x) { return __uint_as_float(x << 16); }
DEV float bfhi(u32 x) { return __uint_as_float(x & 0xffff0000u); }
DEV u32 pk(float a, float b) { f32x2 v = {a, b}; return __builtin_bit_cast(u32, __builtin_convertvector(v, bf16x2)); }
DEV float rbf(float a) { return bflo(pk(a, a)); }
DEV bf16x8 asfrag(u32x4 v) { return __builtin_bit_cast(bf16x8, v); }
DEV u32 opq(u32 a) { asm volatile("" : "+v"(a)); return a; }
DEV u32 opqs(u32 a) { asm volatile("" : "+s"(a)); return a; }
DEV u32x4 lds128(u32 a) { return *(LAS u32x4*)(size_t)a; }
DEV u32x2 lds64v(u32 a) { return *(volatile LAS u32x2*)(size_t)a; }
DEV u32x2 ldstr(u32 a) { return __builtin_bit_cast(u32x2, __builtin_amdgcn_ds_read_tr16_b64_v4i16((LAS s16x4*)(size_t)a)); }
DEV u32x4 cat(u32x2 a, u32x2 b) { return u32x4{a.x, a.y, b.x, b.y}; }
DEV void sts128(u32 a, u32x4 v) { *(LAS u32x4*)(size_t)a = v; }
DEV void sts64(u32 a, u32x2 v) { *(LAS u32x2*)(size_t)a = v; }
DEV void sts32(u32 a, u32 v) { *(LAS u32*)(size_t)a = v; }
DEV void sts16(u32 a, u32 v) { *(LAS u16*)(size_t)a = (u16)v; }
DEV void glds(u32x4 srd, u32 lds, u32 voff, u32 soff) {
  asm volatile("s_mov_b32 m0, %0\n\ts_nop 0\n\tbuffer_load_dwordx4 %1, %2, %3 offen lds" ::"s"(lds), "v"(voff), "s"(srd), "s"(soff) : "memory");
}
DEV void ald128(u32x4& d, u32x4 srd, u32 voff, u32 soff) {
  asm volatile("buffer_load_dwordx4 %0, %1, %2, %3 offen" : "=v"(d) : "v"(voff), "s"(srd), "s"(soff) : "memory");
}
DEV void ald128c(u32x4& d, u32x4 srd, u32 voff, u32 soff) {
  asm volatile("buffer_load_dwordx4 %0, %1, %2, %3 offen sc1" : "=v"(d) : "v"(voff), "s"(srd), "s"(soff) : "memory");
}
DEV void ald32c(u32& d, u32x4 srd, u32 voff, u32 soff) {
  asm volatile("buffer_load_dword %0, %1, %2, %3 offen sc1" : "=v"(d) : "v"(voff), "s"(srd), "s"(soff) : "memory");
}
DEV void st128c(u32x4 d, u32x4 srd, u32 voff, u32 soff) {
  asm volatile("buffer_store_dwordx4 %0, %1, %2, %3 offen sc1\n\ts_nop 1" ::"v"(d), "v"(voff), "s"(srd), "s"(soff) : "memory");
}
#define SB() __builtin_amdgcn_sched_barrier(0)
#define BAR() do { SB(); asm volatile("s_barrier" ::: "memory"); SB(); } while (0)
#define WAITV(n) asm volatile("s_waitcnt vmcnt(" #n ")" ::: "memory")
#define WAITL(n) asm volatile("s_waitcnt lgkmcnt(" #n ")" ::: "memory")
#define PIN(x) asm volatile("" : "+v"(x))
#define MFMA16(a, b, c) __builtin_amdgcn_mfma_f32_16x16x32_bf16(asfrag(a), asfrag(b), c, 0, 0, 0)


DEV void bump(u32x4 ws) {
  asm volatile("s_mov_b64 exec, 1\n\tbuffer_atomic_add %0, off, %1, %2 sc1\n\ts_mov_b64 exec, -1" ::"v"(1u), "s"(ws), "s"(WS_HB) : "memory");
}
DEV void set_flag(u32x4 ws, u32 soff, u32 one) {
  asm volatile("s_mov_b64 exec, 1\n\tbuffer_store_dword %0, off, %1, %2 sc1\n\ts_mov_b64 exec, -1" ::"v"(one), "s"(ws), "s"(soff) : "memory");
}
DEV void poll_flags(u32x4 ws, u32 off, u32 v) {
  if (__builtin_expect(!__all(v == 1u), 0)) {
#pragma unroll 1
    for (int n = 0; n < SPIN_MAX; ++n) {
      if (blockIdx.x == LEADER) bump(ws);
      asm volatile("s_sleep 2\n\tbuffer_load_dword %0, %1, %2, 0 offen sc1\n\ts_waitcnt vmcnt(0)" : "=&v"(v) : "v"(off), "s"(ws) : "memory");
      if (__all(v == 1u)) break;
    }
  }
}

template <class Fn, int... I>
DEV void repeat_(Fn&& f, std::integer_sequence<int, I...>) { (f(std::integral_constant<int, I>{}), ...); }
template <int N, class Fn>
DEV void repeat(Fn&& f) { repeat_(f, std::make_integer_sequence<int, N>{}); }
DEV void pair_sync(u32 mine, u32 other, u32 n) {
  *(volatile LAS u32*)(size_t)mine = n;
  while (*(volatile LAS u32*)(size_t)other < n) {}
  asm volatile("" ::: "memory");
}
DEV void fma4(float (&d)[4], const float (&a)[4], float b, const float (&c)[4]) {
  asm("v_fma_f32 %0, %4, %8, %9\n\tv_fma_f32 %1, %5, %8, %10\n\tv_fma_f32 %2, %6, %8, %11\n\tv_fma_f32 %3, %7, %8, %12"
      : "=&v"(d[0]), "=&v"(d[1]), "=&v"(d[2]), "=&v"(d[3])
      : "v"(a[0]), "v"(a[1]), "v"(a[2]), "v"(a[3]), "v"(b), "v"(c[0]), "v"(c[1]), "v"(c[2]), "v"(c[3]));
}
DEV u32 pka(float a, float b) { u32 r; asm("v_cvt_pk_bf16_f32 %0, %1, %2" : "=v"(r) : "v"(a), "v"(b)); return r; }

DEV float exp_f32(float x) {
  const float hi = x * 1.4426950408889634f, lo = __builtin_fmaf(x, 1.925963033500011e-8f, __builtin_fmaf(x, 1.4426950408889634f, -hi));
  const float e = __builtin_amdgcn_exp2f(hi);
  return __builtin_fmaf(e, lo * 0.6931471805599453f, e);
}
DEV f32x2 cexp2(float e2, float rev) {
  const float m = __builtin_amdgcn_exp2f(e2), f = __builtin_amdgcn_fractf(rev);
  f32x2 z = {m * __builtin_amdgcn_cosf(f), m * __builtin_amdgcn_sinf(f)};
  return z;
}
#define CM_MUL(t, a, z) "v_pk_mul_f32 " t ", " a ", " z " op_sel_hi:[1,0]\n\t"
#define CM_FMA(o, a, z, t) "v_pk_fma_f32 " o ", " a ", " z ", " t " op_sel:[1,1,0] op_sel_hi:[0,1,1] neg_lo:[1,0,0]\n\t"
DEV void cmul2(f32x2& p, f32x2 a, f32x2 zp, f32x2& q, f32x2 c, f32x2 zq) {
  asm(CM_MUL("%0", "%2", "%3") CM_MUL("%1", "%4", "%5") CM_FMA("%0", "%2", "%3", "%0") CM_FMA("%1", "%4", "%5", "%1")
      : "=&v"(p), "=&v"(q) : "v"(a), "v"(zp), "v"(c), "v"(zq));
}
template <int R>
DEV void tstep(u32 adr, f32x2& p, f32x2 a, f32x2 zp, f32x2& q, f32x2 c, f32x2 zq) {
  u32 t, u;
  f32x2 pn, qn;
  asm volatile("v_cvt_pk_bf16_f32 %2, %5, %6\n\tv_cvt_pk_bf16_f32 %3, %7, %8\n\t" CM_MUL("%0", "%9", "%10") CM_MUL("%1", "%11", "%12")
               CM_FMA("%0", "%9", "%10", "%0") CM_FMA("%1", "%11", "%12", "%1") "ds_write2st64_b32 %4, %2, %3 offset0:%13 offset1:%14"
               : "=&v"(pn), "=&v"(qn), "=&v"(t), "=&v"(u)
               : "v"(adr), "v"(p.x), "v"(p.y), "v"(q.x), "v"(q.y), "v"(a), "v"(zp), "v"(c), "v"(zq), "i"(17 * (R >> 3)), "i"(17 * ((R + 16) >> 3))
               : "memory");
  p = pn; q = qn;
}

__global__ __attribute__((amdgpu_waves_per_eu(2, 2))) __launch_bounds__(512, 2) void fused_kernel(
    const void* a_x, const void* a_logA, const void* a_Aim, const void* a_Cre, const void* a_Cim, const void* a_logdt,
    const void* a_Dsk, const void* a_w1, const void* a_b1, const void* a_w2, const void* a_b2, const void* a_gw, void* a_out,
    void* a_ws) {
  u64x8 ka; u64x4 kb; u64x2 kc;
  asm volatile("s_load_dwordx16 %0, %3, 0x0\n\ts_load_dwordx8 %1, %3, 0x40\n\ts_load_dwordx4 %2, %3, 0x60"
               : "=&s"(ka), "=&s"(kb), "=&s"(kc) : "s"(__builtin_amdgcn_kernarg_segment_ptr()));
  const int tid = threadIdx.x, l = tid & 63;
  const int w = __builtin_amdgcn_readfirstlane(tid >> 6), v = w & 3;
  const int b = blockIdx.x, xcd = b & 7, r = b >> 3;
  u32 pf;
  {
    u64 pc;
    asm volatile("s_getpc_b64 %0" : "=s"(pc));
    asm volatile("global_load_dword %0, %1, %2" : "=v"(pf) : "v"(min(w * 32 + r, CODE_LINES - 1) * 128), "s"(pc & ~127ull) : "memory");
  }
  const int pl = r >> 3, nt = r & 7;
  const bool spare = nt == 7;
  const int panel = (xcd >> 1) * 4 + pl, chunk = (xcd & 1) * 7 + (spare ? 0 : nt);
  const int mt = xcd * 4 + pl;
  const int pair = xcd * 32 + r, dl = v >> 1;
  const int hh = l >> 5, blk = (l >> 4) & 1, q4 = (l & 15) >> 2, p4 = l & 3, l15 = l & 15, qq = l >> 4;
  const u32 po = opq(pair * 512 + (tid & 255) * 2), pdo = opq(pair * 4 + dl * 2), l16 = opq(l * 16);
  const u32 xv0 = opq((l >> 3) * 1024 + (((l & 7) ^ (l >> 4)) << 4)), xv1 = opq(xv0 ^ 64);
  const u32 w1v = opq((l >> 4) * (F * 2) + (((l & 15) ^ ((l >> 4) << 1)) << 4)), w1u = opq(w1v ^ 128);
  const int ks0 = (nt + 2 * pl + (xcd >> 1)) & 7;
  const u32 xs0 = (panel * 128 + 32 * v) * 1024, ws0 = 16 * v * (F * 2) + chunk * 256;
  const u32 v4k = opqs(v * 4096);
  u32 sx[4], sw[4];
  auto dxs = [&](int s) { return opqs(ks0 + s == 7 ? -7 * 128 : 128); };
  auto dws = [&](int s) { return opqs(ks0 + s == 7 ? -7 * (64 * F * 2) : 64 * F * 2); };
  u32 fv[5];
#pragma unroll
  for (int i = 0; i < 4; ++i) fv[i] = opq(((i & 1) ? xv1 : xv0) + xs0 + ks0 * 128 + i * 7168);
  fv[4] = opq(w1v + ws0 + ks0 * (64 * F * 2));
  const u32 m0a = opqs(g1buf(0) + v * 4096), m0b = opqs(g1buf(0) + 16384 + v * 4096);
  asm volatile("s_waitcnt lgkmcnt(0)" : "+s"(ka), "+s"(kb), "+s"(kc) :: "memory");
  u32 ila, iai, icr, ici, idt;
  asm volatile("global_load_ushort %4, %6, %11\n\tglobal_load_ushort %0, %5, %7\n\tglobal_load_ushort %1, %5, %8\n\t"
               "global_load_ushort %2, %5, %9\n\tglobal_load_ushort %3, %5, %10"
               : "=&v"(ila), "=&v"(iai), "=&v"(icr), "=&v"(ici), "=&v"(idt)
               : "v"(po), "v"(pdo), "s"(ka[1]), "s"(ka[2]), "s"(ka[3]), "s"(ka[4]), "s"(ka[5]) : "memory");
  if (w >= 4)
    asm volatile("s_mov_b32 m0, %7\n\ts_nop 0\n\tglobal_load_lds_dwordx4 %0, %5\n\tglobal_load_lds_dwordx4 %1, %5 offset:1024\n\t"
                 "global_load_lds_dwordx4 %2, %5 offset:2048\n\tglobal_load_lds_dwordx4 %3, %5 offset:3072\n\t"
                 "s_mov_b32 m0, %8\n\ts_nop 0\n\tglobal_load_lds_dwordx4 %4, %6"
                 ::"v"(fv[0]), "v"(fv[1]), "v"(fv[2]), "v"(fv[3]), "v"(fv[4]), "s"(ka[0]), "s"(ka[7]), "s"(m0a), "s"(m0b) : "memory");
  const u32x4 wsd = mksrd(kc[1], WS_END);
  if (b == LEADER && w < 4) {
#pragma unroll
    for (u32 i = 0; i < 4; ++i) st128c(u32x4{0, 0, 0, 0}, wsd, (l >> 2) * 128 + (l & 3) * 16, (w * 4 + i) * 2048);
  }
  const u32x4 rx = mksrd(ka[0], L * D * 2), rw1 = mksrd(ka[7], spare ? 0 : D * F * 2);
#define ISSUE(lds, va, vb, vc, vd, srd, so, dd)                                                                             \
  asm volatile("s_add_u32 m0, %8, %9\n\ts_nop 0\n\t"                                                                 \
               "buffer_load_dwordx4 %4, %6, %0 offen lds\n\ts_add_u32 %0, %0, %7\n\t"                                \
               "buffer_load_dwordx4 %5, %6, %1 offen offset:1024 lds\n\ts_add_u32 %1, %1, %7\n\t"                    \
               "buffer_load_dwordx4 %10, %6, %2 offen offset:2048 lds\n\ts_add_u32 %2, %2, %7\n\t"                    \
               "buffer_load_dwordx4 %11, %6, %3 offen offset:3072 lds\n\ts_add_u32 %3, %3, %7"                        \
               : "+s"(so[0]), "+s"(so[1]), "+s"(so[2]), "+s"(so[3])                                                   \
               : "v"(va), "v"(vb), "s"(srd), "s"(dd), "s"(v4k), "i"(lds), "v"(vc), "v"(vd) : "memory", "scc")
#define SETM0(lds) asm volatile("s_add_u32 m0, %0, %1\n\ts_nop 0" ::"s"(v4k), "i"(lds) : "memory", "scc")
#define ISSUE1(i, va, srd, so, dd) \
  asm volatile("buffer_load_dwordx4 %1, %2, %0 offen offset:%4 lds\n\ts_add_u32 %0, %0, %3" : "+s"(so[i]) : "v"(va), "s"(srd), "s"(dd), "i"(1024 * (i)) : "memory", "scc")
#define ISSUEX(s, dd) ISSUE(g1buf(s), xv0, xv1, xv0, xv1, rx, sx, dd)
#define ISSUEW(s, dd) ISSUE(g1buf(s) + 16384, w1v, w1v, w1u, w1u, rw1, sw, dd)
  const rsrc_t ws = mkrsrc(kc[1], WS_END);
  u32 hb0;
  u32x2 bias[4];
  if (w < 4)
    asm volatile("buffer_load_dword %0, off, %6, %7 sc1\n\tglobal_load_dwordx2 %1, %5, %8\n\tglobal_load_dwordx2 %2, %5, %8 offset:32\n\t"
                 "global_load_dwordx2 %3, %5, %8 offset:64\n\tglobal_load_dwordx2 %4, %5, %8 offset:96"
                 : "=&v"(hb0), "=&v"(bias[0]), "=&v"(bias[1]), "=&v"(bias[2]), "=&v"(bias[3])
                 : "v"((chunk * 128 + (w & 1) * 64 + 4 * qq) * 2), "s"(wsd), "s"(WS_HB), "s"(kb[0]) : "memory");
  u32 tb[8];
#pragma unroll
  for (int i = 0; i < 8; ++i) tb[i] = opq(LD_TAB + dl * 64 * TST + (tid & 127) * 4 + i * TST);
  const u32 one = opq(1);
  f32x2 za, zb, c0;
#define ANCHORS(ma, mb, sb)                                                                   \
  {                                                                                                  \
    float la, aim, cre, cim, ldt;                                                                    \
    asm volatile("s_waitcnt vmcnt(5)\n\tv_lshlrev_b32 %4, 16, %9\n\tv_lshlrev_b32 %0, 16, %5\n\tv_lshlrev_b32 %1, 16, %6\n\t" \
                 "v_lshlrev_b32 %2, 16, %7\n\tv_lshlrev_b32 %3, 16, %8"                              \
                 : "=&v"(la), "=&v"(aim), "=&v"(cre), "=&v"(cim), "=&v"(ldt)                         \
                 : "v"(ila), "v"(iai), "v"(icr), "v"(ici), "v"(idt), "v"(pf) : "memory");            \
    if (b == LEADER) {                                 \
      BAR();                                                                                         \
      if (w == 0) bump(wsd);                                                                      \
    }                                                                                                \
                                                                                          \
    const float dt = rbf(exp_f32(ldt));                                                              \
    const float ar = -rbf(exp_f32(la)) * dt * 1.4426950408889634f;           \
    const float ai = aim * dt * 0.15915494309189535f;                       \
    za = cexp2(ma * ar, ma * ai); zb = cexp2(mb * ar, sb * ai);                                      \
    c0 = f32x2{2.f * cre * dt, 2.f * cim * dt};                                                      \
  }
#define TS(R, a, zp, c, zq) tstep<R>(tb[(R) & 7], p, a, zp, q, c, zq);   \
  if (R == EARLYW && !spare) ISSUEW(1, dw1);   \
  if (R == PULL0 && spare) W2PULL(0);   \
  if (R == PULL1 && spare) W2PULL(7168);   \
  if (R == EARLYX) ISSUE(g1buf(1), xv0, xv1, xv0, xv1, rx1, sx, dx1);

  const u32x4 rw2 = mksrd(kb[1], F * D * 2);
  const u32 w2s = (896 * (v >> 1) + 8 * (v & 1)) * 1024 + nt * 128;
  u32 w2v;
  auto w2addr = [&]() { w2v = opq((l >> 3) * 1024 + (((l & 7) ^ ((l >> 4) << 1)) << 4)); };
  auto w2o = [&](int step) { return w2s + step * 16384; };
  auto ldw2 = [&](int step) { glds(rw2, slot(step) + v * 1024, w2v, w2o(step)); };

  if (w < 4) {
    u32 xa[2], wa[4], f2o, wb[4][4], da[2];
    const u32 f2w = opqs(spare ? WS_F4 + mt * 128 + w * 4 : WS_F2 + ((panel * 2 + (w >> 1)) * 2 + (xcd & 1)) * 128 + (2 * nt + (w & 1)) * 4);
    {
      ANCHORS(1.f, 16.f, -16.f)
      f32x2 p = {1.f, 0.f}, q = c0;
#define TA(R) tstep<R>(tb[(R) & 7], p, p, zb, q, q, za);
      TA(0) TA(1) TA(2) TA(3) TA(4) TA(5) TA(6) TA(7) TA(8) TA(9) TA(10) TA(11) TA(12) TA(13) TA(14) TA(15)
      asm volatile("s_waitcnt vmcnt(0)" : "+v"(hb0), "+v"(bias[0]), "+v"(bias[1]), "+v"(bias[2]), "+v"(bias[3]) :: "memory");
      if (b == LEADER && w == 0) bump(wsd);
      WAITL(0);
      BAR();
    }
    u32 hb2;
    const u32 f1w = opqs(w ? WS_END : WS_F1 + ((2 * ks0 + (xcd & 1)) * 4 + (xcd >> 1)) * 128 + pl * 4);
    hb0 = opq(w || b == LEADER ? 1u : hb0);
    auto hb_check = [&]() {
      u32 hv;
      asm volatile("s_waitcnt vmcnt(0)\n\tv_mov_b32 %0, %1" : "=v"(hv) : "v"(hb2) : "memory");
      if (__builtin_expect(!__all(hv != hb0), 0)) {
#pragma unroll 1
        for (int n = 0; n < SPIN_MAX; ++n) {
          asm volatile("s_sleep 4\n\tbuffer_load_dword %0, off, %1, %2 sc1\n\ts_waitcnt vmcnt(0)" : "=&v"(hv) : "s"(wsd), "s"(WS_HB) : "memory");
          if (__all(hv != hb0)) break;
        }
      }
      set_flag(wsd, f1w, one);
    };
    {
      const u32 xa0 = LD_B0 + (64 * (w >> 1) + l15) * 128 + ((qq ^ (l15 >> 1)) << 4);
      const u32 wa0 = LD_B0 + 16384 + (8 * qq + q4) * 256 + (((8 * (w & 1) + (p4 >> 1)) ^ ((q4 | ((qq & 1) << 2)) << 1)) << 4) + 8 * (p4 & 1);
      xa[0] = opq(xa0); xa[1] = opq(xa0 ^ 64);
#pragma unroll
      for (int i = 0; i < 4; ++i) wa[i] = opq(wa0 ^ (i << 5));
    }
    const u32 hbo = opqs(w || b == LEADER ? WS_END : WS_HB);
    auto hb_load = [&]() { asm volatile("buffer_load_dword %0, off, %1, %2 sc1" : "=v"(hb2) : "s"(wsd), "s"(hbo) : "memory"); };
    u32 sya;
    auto addr2 = [&]() {
      sya = opq(LD_SYNC + 16 + w * 4);
      f2o = opq(WS_F2 + (mt * 2 + (w >> 1)) * 128 + (l < 14 ? l : 13) * 4);
      const u32 wb0 = (w >> 1) * 2048 + (4 * qq + q4) * 128 + (((p4 >> 1) | (((2 * qq + (q4 >> 1)) & 3) << 1)) << 4) + 8 * (p4 & 1);
#pragma unroll
      for (int i = 0; i < 4; ++i)
#pragma unroll
        for (int j = 0; j < 4; ++j) wb[i][j] = opq((wb0 ^ (j << 5)) + (i < 3 ? i * 65536 : LD_CH + (w >> 1) * (CHS - 2048)));
      const u32 da0 = LD_FD + (w >> 1) * 16384 + (32 * (w & 1) + l15) * 256 + ((qq ^ (l & 7)) << 4);
      da[0] = opq(da0); da[1] = opq(da0 ^ 64);
    };
    if (!spare) {
      const int tm = w >> 1, fh = w & 1;
      f32x4 acc[4][4], bf[4];
      u32x2 fa[16][8];
      u32x4 fb[16][4];
#pragma unroll
      for (int i = 0; i < 4; ++i) { bf[i] = f32x4{bflo(bias[i].x), bfhi(bias[i].x), bflo(bias[i].y), bfhi(bias[i].y)}; PIN(bf[i]); }
      const u32 hb = opqs(WS_H + ((panel * 2 + tm) * 2 * 112 + 8 * chunk + 4 * fh) * 1024);
      w2addr();
      constexpr int LAG = 11;
      auto g1 = [&](auto G) __attribute__((always_inline)) {
        constexpr int g = G, u = g >> 4, j = g & 15, s = u >> 1, ks = u & 1;
        constexpr int m = g - LAG, mu = m >> 4, ft = (m >> 2) & 3, tt = m & 3;
        if (g < 256 && (g & 31) == 0) { BAR(); }
        if constexpr (m >= 0) {
          const u32x4 a = cat(fa[mu][2 * ft], fa[mu][2 * ft + 1]);
          if (mu == 0) asm volatile("v_mfma_f32_16x16x32_bf16 %0, %1, %2, %3" : "=&v"(acc[ft][tt]) : "v"(a), "v"(fb[0][tt]), "v"(bf[ft]));
          else asm volatile("v_mfma_f32_16x16x32_bf16 %0, %1, %2, %0" : "+v"(acc[ft][tt]) : "v"(a), "v"(fb[mu][tt]));
          SB();
        }
        if constexpr (m == 19) asm volatile("" ::"v"(bf[0]), "v"(bf[1]), "v"(bf[2]), "v"(bf[3]));
        if constexpr (g < 256) {
          const u32 dlt = g1buf(s + 1) - g1buf(s);
          if constexpr (j == 0 || (j >= 3 && j < 6)) fb[u][j ? j - 2 : 0] = lds128(xa[ks] + (j ? j - 2 : 0) * 2048);
          else if constexpr (j < 12) { constexpr int k = j < 3 ? j - 1 : j - 4; fa[u][k] = ldstr(wa[k >> 1] + ks * 8192 + (k & 1) * 1024); }
          else if (s < 7) {
            if (j == 12) { xa[ks] += dlt; PIN(xa[ks]); }
            if (ks && j == 13) { wa[0] += dlt; wa[1] += dlt; PIN(wa[0]); PIN(wa[1]); }
            if (ks && j == 14) { wa[2] += dlt; wa[3] += dlt; PIN(wa[2]); PIN(wa[3]); }
            if constexpr (j == 15 && u == HBL) hb_load();
            if constexpr (j == 15 && u == HBC) hb_check();
          }
          SB();
        }
      };
      repeat<200>(g1);
      repeat<56 + LAG>([&](auto G) __attribute__((always_inline)) { g1(std::integral_constant<int, G + 200>{}); });
      auto act = [&](int j, int tt) {
        u32 h0, h1, h2, h3;
        float t0, t1, t2, t3;
        const f32x4 &c = acc[2 * j][tt], &e = acc[2 * j + 1][tt];
        asm volatile("v_mul_f32 %0, %8, |%8|\n\tv_mul_f32 %4, %9, |%9|\n\tv_mul_f32 %1, %10, |%10|\n\tv_mul_f32 %5, %11, |%11|\n\t"
            "v_mul_f32 %2, %12, |%12|\n\tv_mul_f32 %6, %13, |%13|\n\tv_mul_f32 %3, %14, |%14|\n\tv_mul_f32 %7, %15, |%15|\n\t"
            "v_cvt_pk_bf16_f32 %0, %0, %4\n\tv_cvt_pk_bf16_f32 %1, %1, %5\n\tv_cvt_pk_bf16_f32 %2, %2, %6\n\tv_cvt_pk_bf16_f32 %3, %3, %7\n\t"
            "v_pk_max_i16 %0, %0, 0\n\tv_pk_max_i16 %1, %1, 0\n\tv_pk_max_i16 %2, %2, 0\n\tv_pk_max_i16 %3, %3, 0"
            : "=&v"(h0), "=&v"(h1), "=&v"(h2), "=&v"(h3), "=&v"(t0), "=&v"(t1), "=&v"(t2), "=&v"(t3)
            : "v"(c[0]), "v"(c[1]), "v"(c[2]), "v"(c[3]), "v"(e[0]), "v"(e[1]), "v"(e[2]), "v"(e[3]));
        const u32x4 ho = {h0, h1, h2, h3};
        st128c(ho, wsd, l16, hb + ((tt >> 1) * 112 + 2 * j + (tt & 1)) * 1024);
        SB();
      };
#pragma unroll
      for (int i = 0; i < 8; ++i) {
        if (i == G8AT) { BAR(); }
        act(i >> 2, i & 3);
      }
    } else {
      u32x4 gwv, xr[16];
      w2addr();
      hb_load();
      BAR();
      hb_check();
      asm volatile("global_load_dwordx4 %0, %1, %2" : "=v"(gwv) : "v"(l16), "s"(kb[3]) : "memory");
#pragma unroll
      for (int i = 0; i < 16; ++i) ald128(xr[i], rx, l16, (mt * 64 + w * 16 + i) * 1024);
      BAR();
      asm volatile("s_waitcnt vmcnt(0)" : "+v"(gwv) :: "memory");
#pragma unroll
      for (int i = 0; i < 16; ++i) PIN(xr[i]);
      f32x4 s4 = {0.f, 0.f, 0.f, 0.f};
      u32x4 ones = {0x3f803f80u, 0x3f803f80u, 0x3f803f80u, 0x3f803f80u};
      PIN(ones);
      f32x2 gf[4];
#pragma unroll
      for (int j = 0; j < 4; ++j) gf[j] = f32x2{bflo(gwv[j]), bfhi(gwv[j])};
#pragma unroll 1
      for (int h2 = 0; h2 < 2; ++h2) {
        if (b == LEADER && w == 0) bump(wsd);
        f32x4 sr[8];
#pragma unroll
        for (int i = 0; i < 8; ++i) {
          f32x2 t[4];
#pragma unroll
          for (int j = 0; j < 4; ++j) t[j] = f32x2{bflo(xr[i][j]), bfhi(xr[i][j])};
          SB();
#pragma unroll
          for (int j = 0; j < 4; ++j) t[j] *= gf[j];
          SB();
          const u32x4 pr = {pk(t[0].x, t[0].y), pk(t[1].x, t[1].y), pk(t[2].x, t[2].y), pk(t[3].x, t[3].y)};
          xr[i] = xr[i + 8];
          SB();
          sr[i] = MFMA16(ones, pr, (f32x4{0.f, 0.f, 0.f, 0.f}));
          SB();
        }
#pragma unroll
        for (int e = 0; e < 4; ++e) s4[e] = (qq >> 1) != h2 ? s4[e] : (qq & 1) ? sr[4 + e][0] : sr[e][0];
        asm volatile("" ::"v"(sr[0]), "v"(sr[1]), "v"(sr[2]), "v"(sr[3]), "v"(sr[4]), "v"(sr[5]), "v"(sr[6]), "v"(sr[7]));
      }
      u32x4 g4;
#pragma unroll
      for (int e = 0; e < 4; ++e) {
        float t = s4[e];
        t += __int_as_float(__builtin_amdgcn_update_dpp(0, __float_as_int(t), 0x128, 0xf, 0xf, true));
        t += __int_as_float(__builtin_amdgcn_update_dpp(0, __float_as_int(t), 0x124, 0xf, 0xf, true));
        t += __int_as_float(__builtin_amdgcn_update_dpp(0, __float_as_int(t), 0x122, 0xf, 0xf, true));
        t += __int_as_float(__builtin_amdgcn_update_dpp(0, __float_as_int(t), 0x121, 0xf, 0xf, true));
        g4[e] = __float_as_uint(rbf(__builtin_amdgcn_rcpf(1.f + exp_f32(fminf(-rbf(t), 88.f)))));
      }
      PIN(g4);
      BAR();
      if (l15 == 0) st128c(g4, wsd, qq * 16, WS_G + (mt * 64 + w * 16) * 4);
    }
    const int kh = w >> 1, th = w & 1;
    u32x4 hf[56];
    u32x2 wf[224];
    auto rdw = [&](int k) {
      const int s = (k >> 3) * 2 + (k & 1), dt = (k >> 1) & 3;
      wf[k] = ldstr(s < NDMA ? wb[slot(s) >> 16][dt] + (slot(s) & 65535) : wb[3][dt] + (s - NDMA) * 2048);
    };
    u32 f2v;
    static_assert(NFL <= 31 - NPO, "flag before poll");
#pragma unroll
    for (int s = 0; s <= 31; ++s) {
      if (s == NFL) { SB(); addr2(); SB(); asm volatile("s_waitcnt vmcnt(%0)" ::"i"(NFL) : "memory"); set_flag(wsd, f2w, one); }
      if (s == 31 - NPO) { ald32c(f2v, wsd, f2o, 0); }
      if (s < 31) ldw2(s);
    }
    {
      u32 hs = opqs(WS_H + ((mt * 2 + th) * 112 + 56 * kh) * 1024);
      auto ldh = [&](int s) { hf[s] = __builtin_amdgcn_raw_buffer_load_b128(ws, l16 + (s & 3) * 1024, hs + (s >> 2) * 4096, SC1); };
      asm volatile("s_waitcnt vmcnt(%1)" : "+v"(f2v) : "i"(NPO) : "memory");
      poll_flags(wsd, f2o, f2v);
#pragma unroll
      for (int s = 0; s < HRING; ++s) ldh(s);
      SB();
      asm volatile("s_waitcnt vmcnt(%0)" ::"i"(HRING) : "memory");
      pair_sync(sya, sya ^ 4, 1);
      f32x4 o[4][2];
      constexpr int LAG2 = 10;
      constexpr int MT = 224 - 8 * NTL;
      auto mm2 = [&](auto J, auto I) __attribute__((always_inline)) {
        constexpr int j = J, dt = I >> 1, tt = I & 1;
        const u32x4 a = cat(wf[8 * j + 2 * dt], wf[8 * j + 2 * dt + 1]);
        if (j == 0) asm volatile("v_mfma_f32_16x16x32_bf16 %0, %1, %2, 0" : "=&v"(o[dt][tt]) : "v"(a), "v"(hf[tt]));
        else asm volatile("v_mfma_f32_16x16x32_bf16 %0, %1, %2, %0" : "+v"(o[dt][tt]) : "v"(a), "v"(hf[2 * j + tt]));
        SB();
      };
      auto dump = [&](int i) { sts128(da[(i >> 1) & 1] + (i >> 2) * 128 + (i & 1) * 4096, __builtin_bit_cast(u32x4, o[i >> 1][i & 1])); SB(); };
      repeat<224 + LAG2>([&](auto G) __attribute__((always_inline)) {
        constexpr int g = G, m = g - LAG2;
        if (g == 4 * X0 || g == 4 * X2) { if (X0 < 24 && g == 4 * X0) WAITL(0); BAR(); }
        if (g == 4 * X1) { BAR(); }
        if constexpr (m >= 0 && m < MT) mm2(std::integral_constant<int, (m >> 3)>{}, std::integral_constant<int, (m & 7)>{});
        if constexpr (m >= MT) mm2(std::integral_constant<int, 28 - NTL + (m - MT) % NTL>{}, std::integral_constant<int, (m - MT) / NTL>{});
        if constexpr (g < MT) { rdw(g); SB(); }
        if constexpr (g >= MT && g < 224) { constexpr int e = g - MT; rdw(MT + 8 * ((e >> 1) % NTL) + 2 * (e / (2 * NTL)) + (e & 1)); SB(); }
        if constexpr (m >= 0 && (m & 3) == 3 && (m >> 2) + HRING < 56) { ldh((m >> 2) + HRING); SB(); }
        if constexpr (m >= MT + DL && (m - MT - DL) % NTL == NTL - 1) dump((m - MT - DL) / NTL);
      });
      asm volatile("s_nop 7");
      SB();
#pragma unroll
      for (int i = (8 * NTL - DL) / NTL; i < 8; ++i) dump(i);
      WAITL(0);
    }
    BAR();
    if (b == LEADER && w == 0) {
#pragma unroll 1
      for (int n = 0; n < SPIN_MAX; ++n) {
        u32x4 a;
        asm volatile("buffer_load_dwordx4 %0, %1, %2, %3 offen sc1\n\ts_waitcnt vmcnt(0)" : "=&v"(a) : "v"(l * 128), "s"(wsd), "s"(WS_F1) : "memory");
        if (__all((a.x & a.y & a.z & a.w) == 1u)) break;
        bump(wsd);
        __builtin_amdgcn_s_sleep(8);
      }
    }
  } else {
    __builtin_amdgcn_s_setprio(1);
    u32 pf2 = opq(0), dskr;
    u32x4 b2v;
    u32x4 fl[8], f0[8], f1[8];
    f32x4 k0 = {0.f, 0.f, 0.f, 0.f}, k1 = k0;
    auto kmm = [&](int i) { SB(); if (i & 1) k1 = MFMA16(f1[i >> 1], fl[i >> 1], k1); else k0 = MFMA16(f0[i >> 1], fl[i >> 1], k0); SB(); };
    {
      const int rw = 8 * (p4 & 1) + q4;
      const u32 ta0 = LD_B0 + (32 * v + rw) * 128 + (((4 * (xcd & 1) + qq) ^ (rw >> 1)) << 4) + 8 * (p4 >> 1);
      const u32 ta = opq(ta0), tb2 = opq((ta0 ^ 32) + 512);
      const u32 uv = opq((l >> 3) * 16384 + (l & 7) * 16);
      const u32 us = opqs(WS_UT + (16 * ks0 + 8 * (xcd & 1)) * 16384 + (panel * 128 + 32 * v) * 8);
      const u32 dw0 = dws(0), dx1 = dxs(1), dw1 = dws(1);
#pragma unroll
      for (int i = 0; i < 4; ++i) {
        sx[i] = opqs(xs0 + ((ks0 + 1) & 7) * 128 + i * 7168);
        sw[i] = opqs(ws0 + ((ks0 + (i == 0)) & 7) * (64 * F * 2) + i * 13312);
      }
      u32x4 t0, t1;
      const u32x4 rx1 = mksrd(ka[0], spare ? 0 : L * D * 2);
      const u32 pv = opq(((xcd * 16 + pl * 4 + v) * 112 + (l < 28 ? l : 27)) * 128);
#define W2PULL(o) asm volatile("global_load_dword %0, %1, %2\n\tglobal_load_dword %0, %1, %2 offset:3584" : "+v"(pf2) : "v"(pv), "s"(kb[1] + (o)) : "memory")
      const u32 tbase = LD_TAB + dl * 64 * TST + qq * 16;
      const int e2 = p4 < 2 ? p4 : 2;
      const u32 r0o = opq(41 + 5 * (q4 >> 1) + 2 * (q4 & 1) + e2), r1o = opq(51 + 3 * q4 + e2), r1e = opq(32 + ((l15 + ((l15 >> 1) & 1)) >> 1));
      const u32 ra0 = opq(tbase + ((v & 1) ? r0o : 16 + l15) * TST), ra1 = opq(tbase + ((v & 1) ? r1o : r1e) * TST);
      const u32 rl = opq(tbase + l15 * TST), b2o = opq(nt * 128 + (l & 7) * 16);
      SB();
      ANCHORS(2.f, 256.f, 256.f)
      const f32x2 z2 = za, z256 = zb;
      f32x2 z4, z8, z512, z768, c3, p, q, cp, cq;
      cmul2(z4, z2, z2, z512, z256, z256);
      cmul2(z8, z4, z4, z768, z512, z256);
      cmul2(p, c0, z256, c3, c0, z768);
      cmul2(q, c3, z8, cq, c3, z8);
      cp = p;
      PIN(q);
      asm volatile("global_load_ushort %0, %1, %2" : "=v"(dskr) : "v"(pdo), "s"(ka[6]) : "memory");
      asm volatile("global_load_dwordx4 %0, %1, %2" : "=v"(b2v) : "v"(b2o), "s"(kb[2]) : "memory");
      SETM0(g1buf(0) + 16384); ISSUE1(1, w1v, rw1, sw, dw0); ISSUE1(2, w1u, rw1, sw, dw0); ISSUE1(3, w1u, rw1, sw, dw0);
      TS(32, p, z2, q, z4) TS(33, p, z2, q, z4) TS(34, p, z2, c3, z256) cq = q;
      TS(35, p, z2, q, z8) TS(36, p, z2, q, z8) TS(37, p, z2, cq, z256) cq = q;
      TS(38, p, z2, q, z8) TS(39, p, z2, q, z8) TS(40, cp, z256, cq, z256) cq = q; cp = p;
      TS(41, p, z4, q, z8) TS(42, p, z4, q, z8) TS(43, p, z4, cq, z256)
      TS(44, p, z4, q, z8) TS(45, cp, z256, q, z8) TS(46, p, z4, q, z8) TS(47, p, z4, q, z8)
      WAITL(0);
      BAR();
      WAITV(14);
      t0 = cat(ldstr(ta), ldstr(tb2)); t1 = cat(ldstr(ta + 2048), ldstr(tb2 + 2048));
      SB();
#pragma unroll
      for (int ks = 0; ks < 8; ++ks) {
        fl[ks] = lds128(rl + ks * 64); f0[ks] = lds128(ra0 + ks * 64); f1[ks] = lds128(ra1 + ks * 64);
        SB();
        if (ks == 2) {
          st128c(t0, wsd, uv, us);
          st128c(t1, wsd, uv, us + 128);
          SB();
        }
        if (ks >= 3) { kmm(2 * ks - 6); kmm(2 * ks - 5); }
      }
#pragma unroll
      for (int i = 10; i < 16; ++i) kmm(i);
      WAITL(0);
    }
    __builtin_amdgcn_s_setprio(0);
    const int d = pair * 2 + dl, c8 = l & 7, tr = 8 * v + (l >> 3), a5 = l & 31;
    const u32 ch = LD_CH + dl * CHS;
    const u32 f1s = opqs(WS_F1 + ((2 * xcd + (r >> 4)) * 4 + v) * 128);
    u32 sym, syo, zpa, f1o, ka0, ka1, cka, cua, cda, uka, uw0, uqv, dpe, dpo, f3o, avv, gv, ast, aa, d0, d1, oo0, oo1;
    u32x4 kq[6];
    auto addr = [&](int part) {
      if (part == 0) {
        w2addr();
        f1o = opq(f1s + p4 * 4);
        uqv = opq((l >> 1) * 64 + (l & 1) * 16);
      }
      if (part == 1) {
        const u32 ko = opq(2 * (2040 - 512 - 256 * (qq >> 1) - 8 * (qq & 1))), ke = opq(2 * (2044 - 256 - 4 * qq));
        ka0 = opq(ch - 32 * l15 + ((v & 1) ? ko : ke));
      }
      if (part == 3) {
        const u32 ko = opq(2 * (2032 - 1024 - 256 * qq) - 32 * l15), ke = opq(4096 + blk * (KST + 4) + l15 * 4);
        ka1 = opq(ch + ((v & 1) ? ko : ke));
        uw0 = opq(LD_CH + (l & 1) * CHS + 2 * KST + 448 + (64 * v + (l >> 1)) * 16);
      }
      if (part == 4) {
        cka = opq(ch + ((l & 1) ? 2 * (30 - 2 * l15) : KST + 2 * (32 - 2 * l15)) + 16 * qq + 256 * (v & 1));
        cua = opq(ch + 2 * KST + ((l & 1) ? 0 : UST) + 64 * (l15 >> 1) + 16 * qq + 256 * (v & 1));
      }
      if (part == 5) cda = opq(ch + (v & 1) * 8192 + ((l & 1) * 32 + (((l15 >> 1) * 4 + qq) ^ ((l & 1) | ((l15 >> 1) & 2)))) * 16);
      if (part == 6) {
        uka = opq(ch + 2 * KST + 448 + 2048 * (v & 1) + l16);
        dpe = opq(ch + (v & 1) * 1024 + hh * 2048 + (a5 ^ ((a5 >> 2) & 2)) * 16); dpo = opq((dpe ^ 16) + 512);
      }
      if (part == 7) {
        sym = opq(LD_SYNC + v * 4); syo = opq(LD_SYNC + (v ^ 1) * 4);
        zpa = opq(ch + 2 * KST + (v & 1) * UST + (l < 28 ? l : 27) * 16);
      }
      if (part == 8) {
        asm volatile("s_nop 7" : "+v"(k0), "+v"(k1));
        if (v & 1) {
          const u32 a2 = pka(k0[2], k0[2]), a1 = pka(k0[1], k0[1]), am = pka(k0[1], k0[0]), a0 = pka(k0[0], k0[0]);
          const u32 c2 = pka(k1[2], k1[2]), c1 = pka(k1[1], k1[1]), cm2 = pka(k1[1], k1[0]), c0 = pka(k1[0], k1[0]);
          kq[0] = u32x4{a2, a1, am, a0}; kq[1] = u32x4{a2, a2, a1, am};
          kq[2] = u32x4{c2, c2, c1, c1}; kq[3] = u32x4{c1, cm2, c0, c0}; kq[4] = u32x4{c2, c2, c2, c1}; kq[5] = u32x4{c1, c1, cm2, c0};
        } else {
          const u32 c0 = pka(k1[3], k1[2]);
          kq[0] = u32x4{pka(k0[3], k0[2]), pka(k0[1], k0[0]), c0, pka(k1[1], k1[0])};
          kq[1] = u32x4{pka(k1[3], k1[3]), c0, 0, 0};
        }
#pragma unroll
        for (int i = 0; i < 6; ++i) PIN(kq[i]);
      }
    };
    u32 f1v;
    u32x4 wr[56 - NDMA];
    auto ldw2r = [&](int i) { ald128(wr[i], rw2, w2v, w2o(NDMA + i)); };
#define SLOWPF(j) asm volatile("global_load_dword %0, %1, %2" : "+v"(pf2)                                                               \
                               : "v"(((l & 32) * 28 + (j) * 448 + (pl * 4 + v) * 28 + ((l & 31) < 28 ? (l & 31) : 27)) * 1024 + ((3 - ((u32)kb[1] >> 7)) & 7) * 128), "s"(kb[1]) : "memory")
    if (!spare) {
      u32 dx = dxs(2), dw;
#define G1LD(s) BAR(); SETM0(g1buf(s)); ISSUE1(0, xv0, rx, sx, dx); ISSUE1(1, xv1, rx, sx, dx); SB(); addr(2 * (s) - 8); SB();    \
      ISSUE1(2, xv0, rx, sx, dx); ISSUE1(3, xv1, rx, sx, dx); dw = dws(s); dx = dxs(s + 1);   \
      SETM0(g1buf(s) + 16384); ISSUE1(0, w1v, rw1, sw, dw); SB(); addr(2 * (s) - 7); SB();                                     \
      ISSUE1(1, w1v, rw1, sw, dw); ISSUE1(2, w1u, rw1, sw, dw); ISSUE1(3, w1u, rw1, sw, dw);
      static_assert(EARLYX >= EARLYW && EARLYX <= 47 && HBC >= 4, "order of the first loads and stores");
      WAITV(10); G1LD(2) WAITV(10); G1LD(3)
      WAITV(8); G1LD(4) WAITV(8); G1LD(5) WAITV(8); G1LD(6)
      WAITV(8); G1LD(7)
      WAITV(8);
      BAR();
    } else {
      WAITV(0);
      BAR();
#pragma unroll
      for (int i = 0; i < 8; ++i) addr(i);
      BAR();
      SLOWPF(1);
      __builtin_amdgcn_s_sleep(SPW);
    }
    u32x4 uu[2];
    ald32c(f1v, wsd, f1o, 0);
#pragma unroll
    for (int i = 0; i < NP7; ++i) ldw2r(i);
    const u32 uqs = opqs(WS_UT + (pair >> 1) * 16384 + v * 4096 + (pair & 1) * 32), uqs1 = opqs(uqs + 2048);
    if (!spare) { asm volatile("s_waitcnt vmcnt(%0)" ::"i"(NP7 + 1) : "memory"); BAR(); }
    asm volatile("s_waitcnt vmcnt(%2)" : "+v"(f1v), "+v"(pf2) : "i"(NP7) : "memory");
    poll_flags(wsd, f1o, f1v);
    ald128c(uu[0], wsd, uqv, uqs);
    ald128c(uu[1], wsd, uqv, uqs1);
#pragma unroll
    for (int i = NP7; i < 8; ++i) ldw2r(i);
    if (spare) SLOWPF(0);
    addr(8);
    if (v & 1) {
      sts16(zpa + 16, 0);
      sts128(ka0, kq[0]); sts128(ka0 + KST, kq[1]);
      sts128(ka1, kq[2]); sts128(ka1 + 16, kq[3]); sts128(ka1 + KST, kq[4]); sts128(ka1 + KST + 16, kq[5]);
    } else {
      sts64(ka0 + 512, kq[0].xy); sts32(ka0 + 512 + KST + 4, kq[0].x); sts32(ka0 + 512 + KST + 8, kq[0].y);
      sts64(ka0, kq[0].zw); sts64(ka0 + KST, kq[1].xy); sts32(ka0 + KST + 8, kq[0].w);
      sts32(ka1, 0);
    }
    sts128(zpa, u32x4{0, 0, 0, 0});
    sts32(sym, 0); sts32(sym + 16, 0);
    asm volatile("s_waitcnt vmcnt(%2)" : "+v"(uu[0]), "+v"(uu[1]) : "i"(8 - NP7) : "memory");
#pragma unroll
    for (int k = 0; k < 2; ++k) {
      const u32 a = uw0 + 512 * k;
      sts128(a, uu[k]);
      sts16(a + UST + 2, uu[k].x);
      sts32(a + UST + 4, __builtin_amdgcn_alignbyte(uu[k].y, uu[k].x, 2));
      sts64(a + UST + 8, u32x2{__builtin_amdgcn_alignbyte(uu[k].z, uu[k].y, 2), __builtin_amdgcn_alignbyte(uu[k].w, uu[k].z, 2)});
      sts16(a + UST + 16, uu[k].w >> 16);
    }
    WAITL(0);
    BAR();
    u32x4 uk[2];
    {
      f32x4 y[8];
      u32x2 kl[2][8], kr[2][8];
      u32x4 uf[2][8];
      auto rdc = [&](int i, int j, int part) {
        if (part == 0) uf[i & 1][j] = lds128(cua + 64 * i + 512 * j);
        if (part == 1) kl[i & 1][j] = lds64v(cka + 64 * i + 512 * (7 - j));
        if (part == 2) kr[i & 1][j] = lds64v(cka + 64 * i + 512 * (7 - j) + 8);
      };
#pragma unroll
      for (int j = 0; j < 12; ++j) rdc(0, j / 3, j % 3);
      SB();
#pragma unroll
      for (int i = 0; i < 4; ++i) {
        int cnt = 0;
#pragma unroll
        for (int m = 0; m < 8; ++m) {
#pragma unroll
          for (int k = 0; k <= 2 * m; ++k) {
            const int de = k <= m ? k : m, si = k <= m ? m : 2 * m - k, t = de + si;
            if (t <= 7) {
              const u32x4 kf = cat(kl[i & 1][de], kr[i & 1][de]);
              if (i == 0 && si == (t + 1) / 2) asm volatile("v_mfma_f32_16x16x32_bf16 %0, %1, %2, 0" : "=&v"(y[t]) : "v"(kf), "v"(uf[0][si]));
              else asm volatile("v_mfma_f32_16x16x32_bf16 %0, %1, %2, %0" : "+v"(y[t]) : "v"(kf), "v"(uf[i & 1][si]));
              SB();
              if (cnt < 12) { rdc(i, 4 + cnt / 3, cnt % 3); SB(); }
              if (i < 3 && cnt >= 16 && cnt < 28) { rdc(i + 1, (cnt - 16) / 3, (cnt - 16) % 3); SB(); }
              if (i == 3 && (cnt == 12 || cnt == 13)) { uk[cnt - 12] = lds128(uka + 1024 * (cnt - 12)); SB(); }
              if (i == 3 && cnt == 24) { WAITL(0); *(volatile LAS u32*)(size_t)sym = 1; SB(); }
              ++cnt;
            }
          }
        }
      }
      __builtin_amdgcn_s_setprio(1);
      pair_sync(sym, syo, 1);
      asm volatile("s_nop 7" : "+v"(y[5]), "+v"(y[6]), "+v"(y[7]));
#pragma unroll
      for (int t = 0; t < 8; ++t) sts128(cda + (t & 3) * 2048 + (t >> 2) * 1024, __builtin_bit_cast(u32x4, y[t]));
    }
    const u32 ats = opqs(WS_AT + d * 4096 + (v & 1) * 2048);
    const u32 f3w = opqs(WS_F3 + ((v & 1) * 32 + (d >> 4)) * 128 + (d & 15) * 4);
    asm volatile("s_waitcnt vmcnt(0) lgkmcnt(0)" : "+v"(dskr), "+v"(b2v), "+v"(pf2) :: "memory");
    pair_sync(sym, syo, 2);
    {
      f32x4 e[2][2], o[2][2];
#pragma unroll
      for (int k = 0; k < 2; ++k)
#pragma unroll
        for (int h2 = 0; h2 < 2; ++h2) {
          e[k][h2] = __builtin_bit_cast(f32x4, lds128(dpe + k * 4096 + h2 * 8192));
          o[k][h2] = __builtin_bit_cast(f32x4, lds128(dpo + k * 4096 + h2 * 8192));
        }
      SB();
      const float dsk = bf2f(dskr);
#pragma unroll
      for (int k = 0; k < 2; ++k) {
        float ul[4], uh[4], x0[4], x1[4], y0[4], y1[4], t0[4], t1[4], r0[4], r1[4];
#pragma unroll
        for (int j = 0; j < 4; ++j) { ul[j] = bflo(uk[k][j]); uh[j] = bfhi(uk[k][j]); x0[j] = e[k][0][j]; x1[j] = e[k][1][j]; y0[j] = o[k][0][j]; y1[j] = o[k][1][j]; }
        fma4(t0, ul, dsk, x0); fma4(t1, uh, dsk, y0); fma4(r0, x1, 1.f, t0); fma4(r1, y1, 1.f, t1);
        st128c(u32x4{pka(r0[0], r1[0]), pka(r0[1], r1[1]), pka(r0[2], r1[2]), pka(r0[3], r1[3])}, wsd, l16, ats + k * 1024);
      }
    }
    {
      const u32 f3a = opq(WS_F3 + ((mt >> 4) * 32 + nt * 4 + v) * 128 + l15 * 4), f3b = opq(WS_F4 + mt * 128 + p4 * 4);
      f3o = opq(l < 16 ? f3a : f3b);
      avv = opq((l >> 3) * 4096 + c8 * 16); gv = opq(tr * 4);
      ast = opq(LD_AST + (16 * v + (l >> 3)) * 132 + c8 * 16); aa = opq(LD_AST + c8 * 8 * 132 + tr * 2);
      d0 = opq(LD_FD + tr * 256 + (((2 * c8) ^ (tr & 7)) << 4)); d1 = opq(d0 ^ 16);
      oo0 = opq((mt * 64 + tr) * (D * 2) + nt * 128 + c8 * 16); oo1 = opq(oo0 + 32 * D * 2);
    }
    u32 avs[2], gs[2];
#pragma unroll
    for (int k = 0; k < 2; ++k) { avs[k] = opqs(WS_AT + (nt * 64 + 16 * v + 8 * k) * 4096 + mt * 128); gs[k] = opqs(WS_G + mt * 256 + k * 128); }
    __builtin_amdgcn_s_setprio(0);
    {
      const u32 a = opq(ch + (v & 1) * 1024 + l16);
#pragma unroll
      for (int i = 0; i < 8; ++i) { PIN(wr[i]); sts128(a + i * 2048, wr[i]); }
    }
    __builtin_amdgcn_s_setprio(1);
    WAITV(0);
    set_flag(wsd, f3w, one);
    WAITL(0);
    BAR();
    u32 f3v, gr[2];
    u32x4 av[2];
#pragma unroll
    for (int s = 31; s < NDMA; ++s) ldw2(s);
    WAITV(3);
    ald32c(f3v, wsd, f3o, 0);
    WAITV(1);
    BAR();
    asm volatile("s_waitcnt vmcnt(0)" : "+v"(f3v) :: "memory");
    poll_flags(wsd, f3o, f3v);
#pragma unroll
    for (int k = 0; k < 2; ++k) ald128c(av[k], wsd, avv, avs[k]);
#pragma unroll
    for (int k = 0; k < 2; ++k) ald32c(gr[k], wsd, gv, gs[k]);
    asm volatile("s_waitcnt vmcnt(0)" : "+v"(av[0]), "+v"(av[1]), "+v"(gr[0]), "+v"(gr[1]) :: "memory");
#pragma unroll
    for (int k = 0; k < 2; ++k) {
      sts32(ast + k * 1056, av[k].x); sts32(ast + k * 1056 + 4, av[k].y); sts32(ast + k * 1056 + 8, av[k].z); sts32(ast + k * 1056 + 12, av[k].w);
    }
    float Q[2][2][4], og[2];
    {
      float bb[2][4], zz[4] = {0.f, 0.f, 0.f, 0.f}, ob[2][2][4];
#pragma unroll
      for (int j = 0; j < 4; ++j) { bb[j >> 1][2 * (j & 1)] = bflo(b2v[j]); bb[j >> 1][2 * (j & 1) + 1] = bfhi(b2v[j]); }
#pragma unroll
      for (int k = 0; k < 2; ++k) {
        float o1 = 1.f - __uint_as_float(gr[k]);
        PIN(o1);
        og[k] = rbf(o1);
        fma4(ob[k][0], bb[0], og[k], zz); fma4(ob[k][1], bb[1], og[k], zz);
      }
      WAITL(0);
      BAR();
      u32 ar[2][8];
#pragma unroll
      for (int k = 0; k < 2; ++k)
#pragma unroll
        for (int j = 0; j < 8; ++j) ar[k][j] = *(LAS u16*)(size_t)(aa + j * 132 + 64 * k);
      SB();
#pragma unroll
      for (int k = 0; k < 2; ++k)
#pragma unroll
        for (int h2 = 0; h2 < 2; ++h2) {
          float af[4];
#pragma unroll
          for (int j = 0; j < 4; ++j) af[j] = bf2f(ar[k][4 * h2 + j]);
          fma4(Q[k][h2], af, __uint_as_float(gr[k]), ob[k][h2]);
#pragma unroll
          for (int j = 0; j < 4; ++j) PIN(Q[k][h2][j]);
        }
    }
    BAR();
    f32x4 pe[2][2], po2[2][2];
#pragma unroll
    for (int k = 0; k < 2; ++k) {
#pragma unroll
      for (int h2 = 0; h2 < 2; ++h2) {
        pe[k][h2] = __builtin_bit_cast(f32x4, lds128(d0 + k * 8192 + h2 * 16384));
        po2[k][h2] = __builtin_bit_cast(f32x4, lds128(d1 + k * 8192 + h2 * 16384));
      }
      SB();
    }
#pragma unroll
    for (int k = 0; k < 2; ++k) {
      const f32x4 qa = {Q[k][0][0], Q[k][0][1], Q[k][0][2], Q[k][0][3]}, qb = {Q[k][1][0], Q[k][1][1], Q[k][1][2], Q[k][1][3]};
      const f32x4 e = (pe[k][0] + pe[k][1]) * og[k] + qa, o = (po2[k][0] + po2[k][1]) * og[k] + qb;
      const u32x4 res = {pk(e.x, e.y), pk(e.z, e.w), pk(o.x, o.y), pk(o.z, o.w)};
      asm volatile("global_store_dwordx4 %0, %1, %2 sc1 nt\n\ts_nop 1" ::"v"(k ? oo1 : oo0), "v"(res), "s"(kc[0]) : "memory");
      SB();
    }
  }
}

extern "C" void kernel_launch(void* const* d_in, const int* in_sizes, int n_in, void* d_out, int out_size, void* d_ws,
                              size_t ws_size, hipStream_t stream) {
  (void)hipFuncSetAttribute((const void*)fused_kernel, hipFuncAttributeMaxDynamicSharedMemorySize, LDS_BYTES);
  fused_kernel<<<256, 512, LDS_BYTES, stream>>>(d_in[0], d_in[1], d_in[2], d_in[3], d_in[4], d_in[5], d_in[6], d_in[8], d_in[9],
                                                d_in[10], d_in[11], d_in[12], d_out, d_ws);
}
```
